```python
import torch

def _softmax(x, axis=-1):
    return torch.softmax(x, dim=axis)
def _gelu(x):
    return torch.nn.functional.gelu(x, approximate="tanh")
def _silu(x):
    return torch.nn.functional.silu(x)
def _sigmoid(x):
    return torch.sigmoid(x)
def _layernorm(x, axis=-1, eps=1e-5):
    m = x.mean(dim=axis, keepdim=True)
    v = x.var(dim=axis, keepdim=True, unbiased=False)
    return (x - m) / torch.sqrt(v + eps)
def _scatter_add(tgt, idx, src, axis=0):
    return tgt.clone().index_add_(axis, idx, src)
def _segment_sum(src, seg, n):
    out = torch.zeros((n,) + src.shape[1:], dtype=src.dtype)
    return out.index_add_(0, seg, src)
def _pad(x, pw):
    flat = tuple(v for pair in reversed(pw) for v in pair)
    return torch.nn.functional.pad(x, flat)

kernel_name = 'gated_mix__ssm_conv_h3__grouped_kv_mqa__prompt'

def setup_inputs():

    _w = (lambda a: (lambda t: t.to(torch.bfloat16) if t.is_floating_point() else t)(torch.as_tensor(a)))
    def _A_setup():
        B, S, D = 1, 2048, 1024
        ks = 4
        return {
            "q": _w((lambda *s: torch.randn(*s, dtype=torch.bfloat16) * 0.05)(B, S, D)),
            "k": _w((lambda *s: torch.randn(*s, dtype=torch.bfloat16) * 0.4)(B, S, D)),
            "v": _w((lambda *s: torch.randn(*s, dtype=torch.bfloat16) * 0.4)(B, S, D)),
            "shift_k": _w((lambda *s: torch.randn(*s, dtype=torch.bfloat16) * 0.4)(D, ks) * 0.5),
            "ssm_k": _w((lambda *s: torch.randn(*s, dtype=torch.bfloat16) * 0.8)(D, S) * (1.0 / S ** 0.5)),
            "ssm_D": _w((lambda *s: torch.randn(*s, dtype=torch.bfloat16) * 0.4)(D)),
        }
    _A_in = _A_setup()

    def _B_setup():
        B, S, D, H = 1, 2048, 1024, 32
        Dh = D // H
        return {
            "x": _w((lambda *s: torch.randn(*s, dtype=torch.bfloat16) * 0.05)(B, S, D)),
            "wq": _w((lambda *s: torch.randn(*s, dtype=torch.bfloat16) * 0.8)(D, H * Dh)),
            "wk": _w((lambda *s: torch.randn(*s, dtype=torch.bfloat16) * 0.8)(D, Dh)),
            "wv": _w((lambda *s: torch.randn(*s, dtype=torch.bfloat16) * 0.05)(D, Dh)),
            "wo": _w((lambda *s: torch.randn(*s, dtype=torch.bfloat16) * 0.05)(H * Dh, D)),
        }
    _B_in = _B_setup()

    return {
        "A_q": _A_in["q"],
        "A_k": _A_in["k"],
        "A_v": _A_in["v"],
        "A_shift_k": _A_in["shift_k"],
        "A_ssm_k": _A_in["ssm_k"],
        "A_ssm_D": _A_in["ssm_D"],
        "B_x": _B_in["x"],
        "B_wq": _B_in["wq"],
        "B_wk": _B_in["wk"],
        "B_wv": _B_in["wv"],
        "B_wo": _B_in["wo"],
        "gate_w": _w((lambda *s: torch.randn(*s, dtype=torch.bfloat16) * 0.05)(1024) * 16.0),
    }

def reference(A_q, A_k, A_v, A_shift_k, A_ssm_k, A_ssm_D, B_x, B_wq, B_wk, B_wv, B_wo, gate_w):
    def _A_ref(q, k, v, shift_k, ssm_k, ssm_D):
        B, S, D = q.shape
        ks = shift_k.shape[-1]
        k_pad = _pad(k, ((0, 0), (ks - 1, 0), (0, 0)))
        k_shift = torch.zeros_like(k)
        for t in range(ks):
            k_shift = k_shift + k_pad[:, t:t + S, :] * shift_k[:, ks - 1 - t][None, None, :]
        kv = k_shift * v
        L = 2 * S
        kv_pad = _pad(kv, ((0, 0), (0, S), (0, 0)))
        kf_pad = _pad(ssm_k, ((0, 0), (0, S)))
        kv_f = (lambda x, axes=(-2,-1): torch.fft.rfft2(x.float(), dim=axes))(kv_pad[:, None, :, :], axes=(1, 2))
        kf_f = (lambda x, axes=(-2,-1): torch.fft.rfft2(x.float(), dim=axes))(kf_pad[:, None, :], axes=(1, 2))
        kf_f = (lambda x, *d: x.permute(*d))(kf_f, 1, 2, 0)
        y_f = kv_f * kf_f[None, :, :, :]
        y = (lambda x, s, axes=(-2,-1): torch.fft.irfft2(x, s=s, dim=axes).to(torch.bfloat16))(y_f, (1, L), axes=(1, 2))[:, 0, :S, :]
        y = y + kv * ssm_D[None, None, :]
        return q * y

    def _B_ref(x, wq, wk, wv, wo):
        B, S, D = x.shape
        H = 32
        Dh = D // H
        q = (lambda x, *d: x.permute(*d))((x @ wq).reshape(B, S, H, Dh), 0, 2, 1, 3)
        k = x @ wk
        v = x @ wv
        scores = torch.einsum("bhqd,bkd->bhqk", q, k) * (Dh ** -0.5)
        p = _softmax(scores, axis=-1)
        o = torch.einsum("bhqk,bkd->bhqd", p, v)
        return (lambda x, *d: x.permute(*d))(o, 0, 2, 1, 3).reshape(B, S, H * Dh) @ wo

    x = A_q
    g = _sigmoid((x * gate_w).sum(-1, keepdims=True))
    a = _A_ref(x, A_k, A_v, A_shift_k, A_ssm_k, A_ssm_D)
    b = _B_ref(x, B_wq, B_wk, B_wv, B_wo)
    return g * a + (1 - g) * b
```

```cpp
#include <hip/hip_runtime.h>
#include <hip/hip_bf16.h>
#include <stdint.h>

typedef unsigned short u16;
constexpr int S = 2048, D = 1024, H = 32, DH = 32;

__device__ __forceinline__ float b2f(u16 v) { return __uint_as_float(((unsigned)v) << 16); }
__device__ __forceinline__ u16 f2b(float f) { __hip_bfloat16 h = __float2bfloat16(f); return *reinterpret_cast<u16*>(&h); }
__device__ __forceinline__ float rb(float f) { return b2f(f2b(f)); }

__global__ void k_proj(const u16* x, const u16* w, u16* out, int N) {
    int n = blockIdx.x * blockDim.x + threadIdx.x; int r = blockIdx.y;
    if (n >= N) return;
    float acc = 0.f;
    for (int k = 0; k < D; ++k) acc += b2f(x[r * D + k]) * b2f(w[k * N + n]);
    out[r * N + n] = f2b(acc);
}
__global__ void k_attn(const u16* q, const u16* kk, const u16* vv, u16* o) {
    int idx = blockIdx.x * blockDim.x + threadIdx.x; int r = idx / H, h = idx % H;
    float qr[DH], acc[DH];
#pragma unroll
    for (int d = 0; d < DH; ++d) { qr[d] = b2f(q[r * D + h * DH + d]); acc[d] = 0.f; }
    float m = -1e30f, l = 0.f;
    for (int j = 0; j < S; ++j) {
        float s = 0.f;
#pragma unroll
        for (int d = 0; d < DH; ++d) s += qr[d] * b2f(kk[j * DH + d]);
        s *= 0.17677669529663687f;
        float mn = fmaxf(m, s); float a = __expf(m - mn), p = __expf(s - mn); m = mn;
        l = l * a + p;
#pragma unroll
        for (int d = 0; d < DH; ++d) acc[d] = acc[d] * a + p * b2f(vv[j * DH + d]);
    }
#pragma unroll
    for (int d = 0; d < DH; ++d) o[r * D + h * DH + d] = f2b(acc[d] / l);
}
__global__ void k_kv(const u16* k, const u16* v, const u16* sk, u16* kv) {
    int c = blockIdx.x * blockDim.x + threadIdx.x; int t = blockIdx.y;
    float acc = 0.f;
    for (int i = 0; i < 4; ++i) { int tt = t - 3 + i; float kx = tt >= 0 ? b2f(k[tt * D + c]) : 0.f; acc = rb(acc + rb(kx * b2f(sk[c * 4 + 3 - i]))); }
    kv[t * D + c] = f2b(acc * b2f(v[t * D + c]));
}
__global__ void k_conv(const u16* kv, const u16* h, const u16* dd, u16* z) {
    int c = blockIdx.x * blockDim.x + threadIdx.x; int t = blockIdx.y;
    float acc = 0.f;
    for (int s = 0; s <= t; ++s) acc += b2f(h[c * S + t - s]) * b2f(kv[s * D + c]);
    z[t * D + c] = f2b(rb(acc) + rb(b2f(kv[t * D + c]) * b2f(dd[c])));
}
__global__ void k_gate(const u16* x, const u16* gw, float* g) {
    int t = blockIdx.x; int l = threadIdx.x; float acc = 0.f;
    for (int c = l; c < D; c += 64) acc += rb(b2f(x[t * D + c]) * b2f(gw[c]));
    for (int o = 32; o; o >>= 1) acc += __shfl_xor(acc, o);
    if (l == 0) g[t] = rb(1.f / (1.f + __expf(-rb(acc))));
}
__global__ void k_fin(const u16* x, const u16* z, const u16* b, const float* g, u16* out) {
    int c = blockIdx.x * blockDim.x + threadIdx.x; int t = blockIdx.y; int i = t * D + c;
    float gg = g[t]; float a = rb(b2f(x[i]) * b2f(z[i]));
    out[i] = f2b(rb(gg * a) + rb(rb(1.f - gg) * b2f(b[i])));
}

extern "C" void kernel_launch(void* const* d_in, const int* in_sizes, int n_in, void* d_out, int out_size, void* d_ws, size_t ws_size, hipStream_t stream) {
    const u16* x = (const u16*)d_in[0]; const u16* k = (const u16*)d_in[1]; const u16* v = (const u16*)d_in[2];
    const u16* sk = (const u16*)d_in[3]; const u16* ssmk = (const u16*)d_in[4]; const u16* ssmd = (const u16*)d_in[5];
    const u16* wq = (const u16*)d_in[7]; const u16* wk = (const u16*)d_in[8]; const u16* wv = (const u16*)d_in[9]; const u16* wo = (const u16*)d_in[10]; const u16* gw = (const u16*)d_in[11];
    char* ws = (char*)d_ws; const size_t T = (size_t)S * D * 2;
    u16* q = (u16*)ws; u16* o = (u16*)(ws + T); u16* kv = (u16*)(ws + 2 * T); u16* z = (u16*)(ws + 3 * T); u16* b = (u16*)(ws + 4 * T);
    u16* kk = (u16*)(ws + 5 * T); u16* vv = kk + S * DH; float* g = (float*)(ws + 6 * T);
    k_proj<<<dim3(D / 256, S), 256, 0, stream>>>(x, wq, q, D);
    k_proj<<<dim3(1, S), 64, 0, stream>>>(x, wk, kk, DH);
    k_proj<<<dim3(1, S), 64, 0, stream>>>(x, wv, vv, DH);
    k_attn<<<S * H / 64, 64, 0, stream>>>(q, kk, vv, o);
    k_proj<<<dim3(D / 256, S), 256, 0, stream>>>(o, wo, b, D);
    k_kv<<<dim3(D / 256, S), 256, 0, stream>>>(k, v, sk, kv);
    k_conv<<<dim3(D / 256, S), 256, 0, stream>>>(kv, ssmk, ssmd, z);
    k_gate<<<S, 64, 0, stream>>>(x, gw, g);
    k_fin<<<dim3(D / 256, S), 256, 0, stream>>>(x, z, b, g, (u16*)d_out);
}
```

```cpp
#include <hip/hip_runtime.h>
#include <stdint.h>

typedef unsigned short u16;
typedef unsigned int u32;
typedef unsigned long long u64;
using bf16x8 = __attribute__((ext_vector_type(8))) short;
using s16x4 = __attribute__((ext_vector_type(4))) short;
using f32x16 = __attribute__((ext_vector_type(16))) float;
using f32x4 = __attribute__((ext_vector_type(4))) float;
using f32x2 = __attribute__((ext_vector_type(2))) float;
using bf16x2 = __attribute__((ext_vector_type(2))) __bf16;
using u32x4 = __attribute__((ext_vector_type(4))) unsigned;
using u32x2 = __attribute__((ext_vector_type(2))) unsigned;
#define LAS __attribute__((address_space(3)))
#define GAS __attribute__((address_space(1)))
typedef LAS char* lptr;

constexpr int S = 2048, D = 1024;
constexpr int NT = 512;

constexpr size_t WS_KVF = 0;
constexpr size_t WS_UF = 4096;
constexpr size_t WS_OF = 12288;
constexpr size_t WS_ZF = 20480;
constexpr size_t WS_FLAG_BYTES = 24576;
constexpr size_t WS_G = 32768;
constexpr size_t WS_KV = 65536;
constexpr size_t WS_UT = 1u << 20;
constexpr size_t WS_O = 5u << 20;
constexpr size_t WS_ZT = 9u << 20;
constexpr size_t WS_TS = 13u << 20;

constexpr int SLOT = 24576;
constexpr int L_MISC = 6 * SLOT;
constexpr int LDS_BYTES = 163840;
constexpr int L_H = 0;
constexpr int H_SEG = 136, H_CS = 1104, H_CH = 2192;
constexpr int L_U = 5 * SLOT;
constexpr int U_G = 2288, U_CH = 4576;
constexpr int L_KV = 2 * SLOT;

constexpr float QSCALE = 0.17677669529663687f * 1.4426950408889634f;
constexpr float SAFE_T2 = 64.f * 64.f;
constexpr float SAFE_V = 1099511627776.f;
constexpr u32 SPIN_CAP = 1u << 16;

struct Params {
    const u16 *x, *k, *v, *sk, *ssmk, *ssmd, *wq, *wk, *wv, *wo, *gw;
    u16* out;
    char* ws;
};

#define SBAR() __builtin_amdgcn_sched_barrier(0)
#define PIN(x) asm volatile("" : "+v"(x))
#define MFMA32(a, b, c) __builtin_amdgcn_mfma_f32_32x32x16_bf16(a, b, c, 0, 0, 0)
#define MFMA16(a, b, c) __builtin_amdgcn_mfma_f32_16x16x32_bf16(a, b, c, 0, 0, 0)
#define WAIT_BAR(N) asm volatile("s_waitcnt vmcnt(%c0) lgkmcnt(0)\n\ts_barrier" ::"i"(N) : "memory")
#define WAIT_VM0() asm volatile("s_waitcnt vmcnt(0)" ::: "memory")

#define STAMP(i) do { } while (0)

__device__ __forceinline__ u32 rfl(u32 v) { return __builtin_amdgcn_readfirstlane(v); }
__device__ __forceinline__ float bflo(u32 v) { return __uint_as_float(v << 16); }
__device__ __forceinline__ float bfhi(u32 v) { return __uint_as_float(v & 0xffff0000u); }
__device__ __forceinline__ u32 pk2(float lo, float hi) {
    f32x2 f = {lo, hi};
    return __builtin_bit_cast(u32, __builtin_convertvector(f, bf16x2));
}
__device__ __forceinline__ void dma(u32 voff, const void* sbase, u32 ldsaddr) {
    asm volatile("s_mov_b32 m0, %2\n\ts_nop 0\n\tglobal_load_lds_dwordx4 %0, %1" ::"v"(voff), "s"(sbase), "s"(ldsaddr) : "memory");
}
__device__ __forceinline__ void dma_dev(u32 voff, const void* sbase, u32 ldsaddr) {
    asm volatile("s_mov_b32 m0, %2\n\ts_nop 0\n\tglobal_load_lds_dwordx4 %0, %1 sc1" ::"v"(voff), "s"(sbase), "s"(ldsaddr) : "memory");
}
__device__ __forceinline__ void st16_dev(u32 voff, const void* sbase, u32x4 v) {
    asm volatile("global_store_dwordx4 %0, %1, %2 sc1\n\ts_nop 1" ::"v"(voff), "v"(v), "s"(sbase) : "memory");
}
__device__ __forceinline__ void st8_dev(u32 voff, const void* sbase, u32x2 v) {
    asm volatile("global_store_dwordx2 %0, %1, %2 sc1" ::"v"(voff), "v"(v), "s"(sbase) : "memory");
}
__device__ __forceinline__ void st4_dev(void* ptr, u32 v) { __hip_atomic_store((GAS u32*)ptr, v, __ATOMIC_RELAXED, __HIP_MEMORY_SCOPE_AGENT); }
__device__ __forceinline__ void st8g_dev(void* ptr, u64 v) { __hip_atomic_store((GAS u64*)ptr, v, __ATOMIC_RELAXED, __HIP_MEMORY_SCOPE_AGENT); }
__device__ __forceinline__ u32 ld4_dev(const void* ptr) { return __hip_atomic_load((GAS const u32*)ptr, __ATOMIC_RELAXED, __HIP_MEMORY_SCOPE_AGENT); }
__device__ __forceinline__ u64 ld8_dev(const void* ptr) { return __hip_atomic_load((GAS const u64*)ptr, __ATOMIC_RELAXED, __HIP_MEMORY_SCOPE_AGENT); }

__device__ __forceinline__ bf16x8 ldsb128(lptr p) { return *(const LAS bf16x8*)p; }
typedef short v4i16_t __attribute__((ext_vector_type(4)));
__device__ __forceinline__ s16x4 ldstr(lptr p) { return __builtin_bit_cast(s16x4, __builtin_amdgcn_ds_read_tr16_b64_v4i16((LAS v4i16_t*)p)); }
__device__ __forceinline__ bf16x8 cat8(s16x4 a, s16x4 b) { return (bf16x8){a[0], a[1], a[2], a[3], b[0], b[1], b[2], b[3]}; }
__device__ __forceinline__ float swap32sum(float v) {
    auto r = __builtin_amdgcn_permlane32_swap(__float_as_uint(v), __float_as_uint(v), false, false);
    return __uint_as_float(r[0]) + __uint_as_float(r[1]);
}
__device__ __forceinline__ float swap32max(float v) {
    auto r = __builtin_amdgcn_permlane32_swap(__float_as_uint(v), __float_as_uint(v), false, false);
    return fmaxf(__uint_as_float(r[0]), __uint_as_float(r[1]));
}

template <int N>
__device__ __forceinline__ void wait_words(const char* base, u32 tag, int lane) {
    for (u32 spins = 0; spins < SPIN_CAP; ++spins) {
        bool ok = true;
        if (N <= 64) ok = ld4_dev(base + (lane & (N - 1)) * 4) == tag;
        else { const u64 v = ld8_dev(base + lane * 8); ok = (u32)v == tag && (u32)(v >> 32) == tag; }
        if (__all(ok)) break;
        __builtin_amdgcn_s_sleep(1);
    }
    __builtin_amdgcn_fence(__ATOMIC_ACQUIRE, "wavefront");
}

template <bool NATURAL_K>
__device__ __forceinline__ void gemm_stage(f32x16& acc, lptr wp0, lptr wp1, const lptr (&xp)[4], int off) {
#pragma unroll
    for (int ks = 0; ks < 4; ++ks) {
        const bf16x8 a = cat8(ldstr(wp0 + off + ks * 4096), ldstr(wp1 + off + ks * 4096));
        const bf16x8 b = ldsb128(xp[ks] + off);
        acc = MFMA32(a, b, acc);
    }
}

template <int I, int N, class F>
__device__ __forceinline__ void static_for(F&& f) {
    if constexpr (I < N) { f(std::integral_constant<int, I>{}); static_for<I + 1, N>(f); }
}

__global__ __launch_bounds__(NT, 2) void fused_kernel(Params p) {
    extern __shared__ __attribute__((aligned(16))) char smem[];
    const lptr lds = (lptr)smem;
    const u32 ldsb = (u32)(size_t)smem;
    const int tid = threadIdx.x, lane = tid & 63;
    const int wid = rfl(tid >> 6);
    const int bid = blockIdx.x;
    const int xcd = bid & 7, ix = bid >> 3;
    const int rt = xcd * 4 + (ix >> 3), hg = ix & 7;
    const u32 TAG = 1u;
    const int r32 = lane & 31, h = lane >> 5;
    STAMP(0);

    const int cg = hg >> 1;
    const bool dmaw = wid >= 1 && wid <= 6;
    const int dw = wid - 1;
    u32 voA[4], voB[4];
    const char *sbA, *sbB;
    u32 stA, stB;
    if (dw < 4) {
#pragma unroll
        for (int i = 0; i < 4; ++i) { const int row = (8 * dw + 2 * i + h) & 31; voA[i] = row * 2048 + ((r32 ^ (row & 15)) * 16); }
        sbA = (const char*)p.x + (size_t)(64 * rt + 32 * (hg & 1)) * 2048; stA = 512;
    } else {
#pragma unroll
        for (int i = 0; i < 4; ++i) voA[i] = ((((dw - 4) * 4 + i) & 7) * 32 + (lane >> 1)) * 64 + (lane & 1) * 16;
        sbA = (const char*)(cg < 2 ? p.wk : p.wv) + (cg & 1) * 32; stA = 256 * 64;
    }
    if (dw < 2) {
#pragma unroll
        for (int i = 0; i < 4; ++i) { const int row = (8 * (4 * dw + i) + (lane >> 3)) & 63; voB[i] = row * 2048 + (((lane & 7) ^ ((row >> 1) & 7)) * 16); }
        sbB = (const char*)p.x + (size_t)(64 * rt) * 2048; stB = 128;
    } else {
#pragma unroll
        for (int i = 0; i < 4; ++i) { const int row = 4 * ((4 * (dw - 2) + i) & 15) + (lane >> 4); voB[i] = row * 2048 + (((lane & 15) ^ (((lane >> 4) << 2) | i)) * 16); }
        sbB = (const char*)p.wq + hg * 256; stB = 64 * 2048;
    }
    const u32 ldsw = ldsb + (dw & 7) * 4096;
    auto issue0 = [&](auto SG) {
        constexpr int sg = SG.value;
        if (dmaw) {
#pragma unroll
            for (int i = 0; i < 4; ++i) {
                if constexpr (sg < 4) dma(voA[i], sbA + (size_t)sg * stA, ldsw + (sg % 6) * SLOT + i * 1024);
                else dma(voB[i], sbB + (size_t)(sg - 4) * stB, ldsw + (sg % 6) * SLOT + i * 1024);
            }
        }
    };
    static_for<0, 6>(issue0);

    const int hd = wid >> 1, rh = wid & 1;
    lptr wp0, wp1, xp[4];
    {
        const int blk = (lane >> 4) & 1, q = (lane & 15) >> 2, pp = lane & 3;
        const int ch = 4 * hd + 2 * blk + (pp >> 1);
        wp0 = lds + 8192 + 256 * (8 * h + q) + 16 * (ch ^ ((q << 2) | (2 * h))) + 8 * (pp & 1);
        wp1 = lds + 8192 + 256 * (8 * h + 4 + q) + 16 * (ch ^ ((q << 2) | (2 * h + 1))) + 8 * (pp & 1);
        const int row = 32 * rh + r32;
#pragma unroll
        for (int ks = 0; ks < 4; ++ks) xp[ks] = lds + row * 128 + 16 * ((2 * ks + h) ^ ((row >> 1) & 7));
    }
    const bool kvw = wid == 0 || wid == 7;
    const int ku = wid == 7 ? 1 : 0;
    f32x4 kvacc = {0.f, 0.f, 0.f, 0.f};
    f32x16 qacc = {};
    float kvstat = 0.f;

    static_for<0, 20>([&](auto SG) {
        constexpr int sg = SG.value;
        constexpr int nout = sg == 0 ? 5 : (sg <= 15 ? 4 : 19 - sg);
        WAIT_BAR(4 * nout);
        if constexpr (sg >= 1 && sg + 5 <= 19) issue0(std::integral_constant<int, sg + 5>{});
        constexpr int off = (sg % 6) * SLOT;
        if constexpr (sg < 4) {
            if (kvw) {
                const int g = lane >> 4, row = 16 * ku + (lane & 15);
                const lptr xr = lds + off + row * 512;
                const lptr wr = lds + off + 16384 + (8 * g + ((lane & 15) >> 2)) * 32 + 8 * (lane & 3);
#pragma unroll
                for (int ks = 0; ks < 8; ++ks) {
                    const bf16x8 xf = ldsb128(xr + 16 * ((4 * ks + g) ^ (row & 15)));
                    const bf16x8 wf = cat8(ldstr(wr + ks * 1024), ldstr(wr + ks * 1024 + 128));
                    if (cg < 2) kvacc = MFMA16(wf, xf, kvacc); else kvacc = MFMA16(xf, wf, kvacc);
                }
                if constexpr (sg == 3) {
                    const int tile = 2 * rt + (hg & 1), c15 = lane & 15;
                    const u32 o = cg < 2 ? (u32)(cg * 1024 + ((g & 1) * 32 + 16 * ku + c15) * 16 + (g >> 1) * 8)
                                         : (u32)(2048 + ku * 1024 + ((g & 1) * 32 + 16 * (cg - 2) + c15) * 16 + (g >> 1) * 8);
                    st8_dev(o, p.ws + WS_KV + (size_t)tile * 4096, u32x2{pk2(kvacc[0], kvacc[1]), pk2(kvacc[2], kvacc[3])});
                    float m;
                    if (cg < 2) {
                        m = kvacc[0] * kvacc[0] + kvacc[1] * kvacc[1] + kvacc[2] * kvacc[2] + kvacc[3] * kvacc[3];
                        m += __shfl_xor(m, 16); m += __shfl_xor(m, 32);
                    } else {
                        m = __uint_as_float(max(max(__float_as_uint(kvacc[0]) & 0x7fffffffu, __float_as_uint(kvacc[1]) & 0x7fffffffu),
                                                max(__float_as_uint(kvacc[2]) & 0x7fffffffu, __float_as_uint(kvacc[3]) & 0x7fffffffu)));
                    }
                    u32 mb = __float_as_uint(m) & 0x7fffffffu;
#pragma unroll
                    for (int o2 = 1; o2 < 64; o2 <<= 1) mb = max(mb, (u32)__shfl_xor((int)mb, o2));
                    kvstat = __uint_as_float(mb);
                }
            }
        } else {
            gemm_stage<true>(qacc, wp0, wp1, xp, off);
            if constexpr (sg == 5) {
                if (kvw) {
                    WAIT_VM0();
                    if (lane == 0) st8g_dev(p.ws + WS_KVF + (size_t)((rt * 8 + hg) * 2 + ku) * 8, ((u64)__float_as_uint(kvstat) << 32) | TAG);
                }
            }
        }
    });
    STAMP(1);
    WAIT_BAR(0);

    bf16x8 qf0, qf1;
    float q2 = 0.f;
    {
        float qs[16];
#pragma unroll
        for (int i = 0; i < 16; ++i) { qs[i] = qacc[i] * QSCALE; q2 = fmaf(qs[i], qs[i], q2); }
        q2 = swap32sum(q2);
        qf0 = __builtin_bit_cast(bf16x8, (u32x4){pk2(qs[0], qs[1]), pk2(qs[2], qs[3]), pk2(qs[4], qs[5]), pk2(qs[6], qs[7])});
        qf1 = __builtin_bit_cast(bf16x8, (u32x4){pk2(qs[8], qs[9]), pk2(qs[10], qs[11]), pk2(qs[12], qs[13]), pk2(qs[14], qs[15])});
    }

    bool fast;
    {
        u32 k0 = 0, k1 = 0, vm = 0;
        for (u32 spins = 0; spins < SPIN_CAP; ++spins) {
            bool ok = true; k0 = 0; k1 = 0; vm = 0;
#pragma unroll
            for (int i = 0; i < 8; ++i) {
                const u64 gv = ld8_dev(p.ws + WS_KVF + (size_t)(i * 64 + lane) * 8);
                ok &= (u32)gv == TAG;
                const u32 val = (u32)(gv >> 32);
                const int c = (lane >> 2) & 3;
                k0 = max(k0, c == 0 ? val : 0u); k1 = max(k1, c == 1 ? val : 0u); vm = max(vm, c >= 2 ? val : 0u);
            }
            if (__all(ok)) break;
            __builtin_amdgcn_s_sleep(1);
        }
        __builtin_amdgcn_fence(__ATOMIC_ACQUIRE, "wavefront");
#pragma unroll
        for (int o2 = 1; o2 < 64; o2 <<= 1) {
            k0 = max(k0, (u32)__shfl_xor((int)k0, o2)); k1 = max(k1, (u32)__shfl_xor((int)k1, o2)); vm = max(vm, (u32)__shfl_xor((int)vm, o2));
        }
        const float k2 = __uint_as_float(k0) + __uint_as_float(k1);
        fast = __all(q2 * k2 <= SAFE_T2 && __uint_as_float(vm) <= SAFE_V);
    }
    const char* kvg = p.ws + WS_KV + wid * 2048;
    const u32 kvl = ldsb + L_KV + wid * 2048;
    const u32 vl16 = lane * 16;
    dma_dev(vl16, kvg, kvl); dma_dev(vl16, kvg + 1024, kvl + 1024);
    dma_dev(vl16, kvg + 16384, kvl + SLOT); dma_dev(vl16, kvg + 16384 + 1024, kvl + SLOT + 1024);
    STAMP(2);

    {
        const int cp = lane & 7, tg = lane >> 3;
        const int c0 = 128 * hg + 16 * wid + 2 * cp, t0 = 64 * rt + 8 * tg;
        u32 kr[11], vr[8];
#pragma unroll
        for (int i = 0; i < 11; ++i) { const int t = t0 - 3 + i; kr[i] = *(const u32*)(p.k + (size_t)max(t, 0) * D + c0); if (i < 3 && t < 0) kr[i] = 0u; }
#pragma unroll
        for (int i = 0; i < 8; ++i) vr[i] = *(const u32*)(p.v + (size_t)(t0 + i) * D + c0);
        const u32x4 sw = *(const u32x4*)(p.sk + c0 * 4);
        const int grow = 64 * rt + 8 * hg + wid;
        const u32x4 gx0 = *(const u32x4*)(p.x + (size_t)grow * D + lane * 8), gx1 = *(const u32x4*)(p.x + (size_t)grow * D + 512 + lane * 8);
        const u32x4 gw0 = *(const u32x4*)(p.gw + lane * 8), gw1 = *(const u32x4*)(p.gw + 512 + lane * 8);
        float u0[8], u1[8];
#pragma unroll
        for (int i = 0; i < 8; ++i) {
            const float a = fmaf(bflo(sw[0]), bflo(kr[i + 3]), fmaf(bfhi(sw[0]), bflo(kr[i + 2]), fmaf(bflo(sw[1]), bflo(kr[i + 1]), bfhi(sw[1]) * bflo(kr[i]))));
            const float b = fmaf(bflo(sw[2]), bfhi(kr[i + 3]), fmaf(bfhi(sw[2]), bfhi(kr[i + 2]), fmaf(bflo(sw[3]), bfhi(kr[i + 1]), bfhi(sw[3]) * bfhi(kr[i]))));
            u0[i] = a * bflo(vr[i]); u1[i] = b * bfhi(vr[i]);
        }
        const u32 uo = (u32)(c0 * 4096 + (2040 - t0) * 2);
        st16_dev(uo, p.ws + WS_UT, u32x4{pk2(u0[7], u0[6]), pk2(u0[5], u0[4]), pk2(u0[3], u0[2]), pk2(u0[1], u0[0])});
        st16_dev(uo + 4096, p.ws + WS_UT, u32x4{pk2(u1[7], u1[6]), pk2(u1[5], u1[4]), pk2(u1[3], u1[2]), pk2(u1[1], u1[0])});
        float gs = 0.f;
#pragma unroll
        for (int i = 0; i < 4; ++i) {
            gs = fmaf(bflo(gx0[i]), bflo(gw0[i]), gs); gs = fmaf(bfhi(gx0[i]), bfhi(gw0[i]), gs);
            gs = fmaf(bflo(gx1[i]), bflo(gw1[i]), gs); gs = fmaf(bfhi(gx1[i]), bfhi(gw1[i]), gs);
        }
#pragma unroll
        for (int o2 = 1; o2 < 64; o2 <<= 1) gs += __shfl_xor(gs, o2);
        if (lane == 0) st4_dev(p.ws + WS_G + grow * 4, __float_as_uint(1.f / (1.f + __expf(-gs))));

        const int cch = wid & 3, cpy = wid >> 2;
        const u16* hrow = p.ssmk + (size_t)(4 * bid + cch) * S;
        const lptr hb = lds + L_H + (cch * H_CH + cpy * H_CS) * 4;
#pragma unroll
        for (int i = 0; i < 5; ++i) {
            const int n = i * 64 + lane;
            if (n < 272) {
                const int seg = n / 34, cc = n - seg * 34;
                const int e = 256 * seg - 16 + 8 * cc;
                const u32x4 a = *(const u32x4*)(hrow + max(e, 0));
                const u32 prev = *(const u32*)(hrow + max(e - 2, 0));
                u32x4 w = a;
                if (cpy) w = u32x4{__builtin_amdgcn_alignbit(a[0], e >= 2 ? prev : 0u, 16), __builtin_amdgcn_alignbit(a[1], a[0], 16),
                                   __builtin_amdgcn_alignbit(a[2], a[1], 16), __builtin_amdgcn_alignbit(a[3], a[2], 16)};
                if (e < 0) w = u32x4{0u, 0u, 0u, 0u};
                *(LAS u32x4*)(hb + n * 16) = w;
            }
        }
        if (lane < 15) *(LAS u32x4*)(lds + L_U + cch * U_CH + cpy * U_G + 2048 + lane * 16) = u32x4{0u, 0u, 0u, 0u};
        WAIT_VM0();
        if (lane == 0) st4_dev(p.ws + WS_UF + ((hg * 8 + wid) * 32 + rt) * 4, TAG);
    }
    STAMP(3);

    f32x16 oacc = {};
    float linv;
    const lptr kvp = lds + L_KV + lane * 16;
    auto side = [&](int s) {
        if (s + 2 < 16) {
            const u32 sl = (u32)((s + 2) % 3) * SLOT;
            dma_dev(vl16, kvg + (size_t)(s + 2) * 16384, kvl + sl); dma_dev(vl16, kvg + (size_t)(s + 2) * 16384 + 1024, kvl + sl + 1024);
        }
        if (s == 12) {
            wait_words<32>(p.ws + WS_UF + (size_t)(bid >> 2) * 128, TAG, lane);
            const int cch = wid & 3, par = wid >> 2;
            const char* ug = p.ws + WS_UT + (size_t)(4 * bid + cch) * 4096 + par * 16;
            const u32 ul = ldsb + L_U + cch * U_CH + par * U_G;
            dma_dev(lane * 32, ug, ul); dma_dev(lane * 32, ug + 2048, ul + 1024);
        }
    };
    if (fast) {
        const short one = ((lane & 1) == ((lane >> 4) & 1)) ? (short)0x3f80 : (short)0;
        bf16x8 ones = {one, one, one, one, one, one, one, one};
        PIN(ones);
        f32x4 lacc = {0.f, 0.f, 0.f, 0.f};
        const f32x16 zero = {};
        WAIT_BAR(0);
        f32x16 sA, sB;
        bf16x8 k0f = ldsb128(kvp), k1f = ldsb128(kvp + 1024);
        sA = MFMA32(k0f, qf0, zero); sA = MFMA32(k1f, qf1, sA);
        k0f = ldsb128(kvp + 4096); k1f = ldsb128(kvp + 4096 + 1024);
        u32x4 pw1 = {0u, 0u, 0u, 0u};
        bf16x8 v1f = {};
        int cur = 0;
#define EX(X, i) X[i] = __builtin_amdgcn_exp2f(X[i])
#define TILE(C, N, VP, KN) do { \
        bf16x8 v0f = ldsb128((VP) + 2048); u32x4 pw0; \
        N = MFMA32(k0f, qf0, zero); EX(C, 0); EX(C, 1); pw0[0] = pk2(C[0], C[1]); EX(C, 2); PIN(C); PIN(pw0); SBAR(); \
        oacc = MFMA32(v1f, __builtin_bit_cast(bf16x8, pw1), oacc); EX(C, 3); pw0[1] = pk2(C[2], C[3]); EX(C, 4); EX(C, 5); PIN(C); PIN(pw0); SBAR(); \
        N = MFMA32(k1f, qf1, N); pw0[2] = pk2(C[4], C[5]); EX(C, 6); EX(C, 7); pw0[3] = pk2(C[6], C[7]); PIN(C); PIN(pw0); SBAR(); \
        k0f = ldsb128(KN); k1f = ldsb128((KN) + 1024); \
        lacc = MFMA16(ones, __builtin_bit_cast(bf16x8, pw1), lacc); EX(C, 8); EX(C, 9); PIN(C); SBAR(); \
        v1f = ldsb128((VP) + 3072); \
        oacc = MFMA32(v0f, __builtin_bit_cast(bf16x8, pw0), oacc); pw1[0] = pk2(C[8], C[9]); EX(C, 10); EX(C, 11); pw1[1] = pk2(C[10], C[11]); EX(C, 12); PIN(C); PIN(pw1); SBAR(); \
        lacc = MFMA16(ones, __builtin_bit_cast(bf16x8, pw0), lacc); EX(C, 13); pw1[2] = pk2(C[12], C[13]); EX(C, 14); EX(C, 15); pw1[3] = pk2(C[14], C[15]); PIN(pw1); SBAR(); \
    } while (0)
        for (int s = 0; s < 16; ++s) {
            side(s);
            const int nxt = cur == 2 * SLOT ? 0 : cur + SLOT;
            const lptr c = kvp + cur, n = kvp + nxt;
            SBAR();
            TILE(sA, sB, c, c + 8192);
            TILE(sB, sA, c + 4096, c + 12288);
            TILE(sA, sB, c + 8192, n);
            TILE(sB, sA, c + 12288, n + 4096);
            cur = nxt;
            WAIT_BAR(0);
        }
        oacc = MFMA32(v1f, __builtin_bit_cast(bf16x8, pw1), oacc);
        lacc = MFMA16(ones, __builtin_bit_cast(bf16x8, pw1), lacc);
        linv = 1.f / ((lane & 16) ? lacc[1] : lacc[0]);
#undef TILE
#undef EX
    } else {
        float m = -__builtin_inff(), l = 0.f;
        const f32x16 zero = {};
        int cur = 0;
        WAIT_BAR(0);
        for (int s = 0; s < 16; ++s) {
            side(s);
            for (int t = 0; t < 4; ++t) {
                const lptr c = kvp + cur + t * 4096;
                f32x16 sc = MFMA32(ldsb128(c), qf0, zero);
                sc = MFMA32(ldsb128(c + 1024), qf1, sc);
                float mx = sc[0];
#pragma unroll
                for (int i = 1; i < 16; ++i) mx = fmaxf(mx, sc[i]);
                const float mn = fmaxf(m, swap32max(mx));
                const float al = __builtin_amdgcn_exp2f(m - mn);
                m = mn;
                float ps = 0.f;
#pragma unroll
                for (int i = 0; i < 16; ++i) { sc[i] = __builtin_amdgcn_exp2f(sc[i] - mn); ps += sc[i]; oacc[i] *= al; }
                l = l * al + ps;
                const u32x4 a = {pk2(sc[0], sc[1]), pk2(sc[2], sc[3]), pk2(sc[4], sc[5]), pk2(sc[6], sc[7])};
                const u32x4 b = {pk2(sc[8], sc[9]), pk2(sc[10], sc[11]), pk2(sc[12], sc[13]), pk2(sc[14], sc[15])};
                oacc = MFMA32(ldsb128(c + 2048), __builtin_bit_cast(bf16x8, a), oacc);
                oacc = MFMA32(ldsb128(c + 3072), __builtin_bit_cast(bf16x8, b), oacc);
            }
            cur = cur == 2 * SLOT ? 0 : cur + SLOT;
            WAIT_BAR(0);
        }
        linv = 1.f / swap32sum(l);
    }
    STAMP(4);
    {
        const char* og = p.ws + WS_O + (size_t)((rt * 32 + hg * 4 + hd) * 2 + rh) * 2048;
        st16_dev(vl16, og, u32x4{pk2(oacc[0] * linv, oacc[1] * linv), pk2(oacc[2] * linv, oacc[3] * linv), pk2(oacc[4] * linv, oacc[5] * linv), pk2(oacc[6] * linv, oacc[7] * linv)});
        st16_dev(vl16, og + 1024, u32x4{pk2(oacc[8] * linv, oacc[9] * linv), pk2(oacc[10] * linv, oacc[11] * linv), pk2(oacc[12] * linv, oacc[13] * linv), pk2(oacc[14] * linv, oacc[15] * linv)});
        WAIT_VM0();
        if (lane == 0) st4_dev(p.ws + WS_OF + (rt * 64 + hg * 8 + wid) * 4, TAG);
    }
    STAMP(5);

    u32 voC[3];
    const char* sbC[3];
    u32 stC[3];
#pragma unroll
    for (int i = 0; i < 3; ++i) {
        const int pc = wid * 3 + i;
        if (pc < 8) { voC[i] = vl16; sbC[i] = p.ws + WS_O + (size_t)rt * 131072 + pc * 1024; stC[i] = 8192; }
        else {
            const int q = pc - 8;
            voC[i] = (4 * q + (lane >> 4)) * 2048 + (((lane & 15) ^ (((lane >> 4) << 2) | (q & 3))) * 16);
            sbC[i] = (const char*)p.wo + hg * 256; stC[i] = 64 * 2048;
        }
    }
    if (wid >= 4) {
        const int hw = wid - 4;
#pragma unroll
        for (int j = 0; j < 3; ++j)
#pragma unroll
            for (int i = 0; i < 4; ++i) {
                const int q = 4 * hw + i;
                dma((4 * q + (lane >> 4)) * 2048 + (((lane & 15) ^ (((lane >> 4) << 2) | i)) * 16), (const char*)p.wo + hg * 256 + (size_t)j * 64 * 2048,
                    ldsb + (2 + j) * SLOT + 8192 + q * 1024);
            }
        wait_words<64>(p.ws + WS_OF + rt * 256, TAG, lane);
#pragma unroll
        for (int j = 0; j < 3; ++j)
#pragma unroll
            for (int i = 0; i < 2; ++i)
                dma_dev(vl16, p.ws + WS_O + (size_t)rt * 131072 + j * 8192 + (2 * hw + i) * 1024, ldsb + (2 + j) * SLOT + (2 * hw + i) * 1024);
    } else {
        const int al = (lane >> 4) & 1, m = lane & 15;
        const lptr ap = lds + L_H + (wid * H_CH + ((m & 1) ? 0 : H_CS) + al * H_SEG + ((m + 2) >> 1) + 4 * h) * 4;
        const lptr bp = lds + L_U + wid * U_CH + h * U_G + 16 * (127 - 16 * al - m);
        f32x16 c0 = {}, c1 = {}, c2 = {}, c3 = {};
#pragma unroll 2
        for (int kp = 0; kp < 16; ++kp) {
            bf16x8 a[4], b[4];
#pragma unroll
            for (int i = 0; i < 4; ++i) {
                const LAS u32* q = (const LAS u32*)(ap + (2 * i * H_SEG + 8 * kp) * 4);
                a[i] = __builtin_bit_cast(bf16x8, (u32x4){q[0], q[1], q[2], q[3]});
                b[i] = ldsb128(bp + 16 * kp - 512 * i);
            }
            c3 = MFMA32(a[0], b[3], c3); c2 = MFMA32(a[0], b[2], c2); c1 = MFMA32(a[0], b[1], c1); c0 = MFMA32(a[0], b[0], c0);
            c3 = MFMA32(a[1], b[2], c3); c2 = MFMA32(a[1], b[1], c2); c1 = MFMA32(a[1], b[0], c1);
            c3 = MFMA32(a[2], b[1], c3); c2 = MFMA32(a[2], b[0], c2);
            c3 = MFMA32(a[3], b[0], c3);
        }
        float y[8][4];
        auto fold = [&](const f32x16& c, int C) {
#pragma unroll
            for (int i = 0; i < 4; ++i) {
                auto x = __builtin_amdgcn_permlane16_swap(__float_as_uint(c[i]), __float_as_uint(c[i + 4]), false, false);
                auto r = __builtin_amdgcn_permlane16_swap(__float_as_uint(c[8 + i]), __float_as_uint(c[12 + i]), false, false);
                if (C == 0) y[0][i] = __uint_as_float(x[0]); else y[2 * C][i] += __uint_as_float(x[0]);
                y[2 * C + 1][i] = __uint_as_float(x[1]) + __uint_as_float(r[0]);
                if (C < 3) y[2 * C + 2][i] = __uint_as_float(r[1]);
            }
        };
        fold(c0, 0); fold(c1, 1); fold(c2, 2); fold(c3, 3);
        const int ch = 4 * bid + wid;
        const float dd = __uint_as_float((u32)p.ssmd[ch] << 16);
        const int tl = 16 * m + 8 * al + 4 * h;
        const char* zg = p.ws + WS_ZT + (size_t)ch * 4096;
#pragma unroll
        for (int c = 0; c < 8; ++c) {
            const int xr = 2044 - 256 * c - tl;
            const u32x2 uu = *(const LAS u32x2*)(lds + L_U + wid * U_CH + ((xr >> 3) & 1) * U_G + (xr >> 4) * 16 + (xr & 4) * 2);
            const float z0 = fmaf(bfhi(uu[1]), dd, y[c][0]), z1 = fmaf(bflo(uu[1]), dd, y[c][1]);
            const float z2 = fmaf(bfhi(uu[0]), dd, y[c][2]), z3 = fmaf(bflo(uu[0]), dd, y[c][3]);
            st8_dev((u32)(256 * c + tl) * 2, zg, u32x2{pk2(z0, z1), pk2(z2, z3)});
        }
        WAIT_VM0();
        if (lane == 0) st4_dev(p.ws + WS_ZF + ch * 4, TAG);
    }
    STAMP(6);

    f32x16 bacc = {};
    {
        WAIT_BAR(0);
        auto issue3 = [&](auto J) {
            constexpr int j = J.value;
#pragma unroll
            for (int i = 0; i < 3; ++i) {
                const u32 dst = ldsb + ((2 + j) % 6) * SLOT + (wid * 3 + i) * 1024;
                if (wid * 3 + i < 8) dma_dev(voC[i], sbC[i] + (size_t)j * stC[i], dst); else dma(voC[i], sbC[i] + (size_t)j * stC[i], dst);
            }
        };
        static_for<3, 6>(issue3);
        const int blk = (lane >> 4) & 1, q = (lane & 15) >> 2, pp = lane & 3;
        const int ch = 4 * hd + 2 * blk + (pp >> 1);
        wp0 = lds + 8192 + 256 * (4 * h + q) + 16 * (ch ^ ((q << 2) | h)) + 8 * (pp & 1);
        wp1 = lds + 8192 + 256 * (8 + 4 * h + q) + 16 * (ch ^ ((q << 2) | (2 + h))) + 8 * (pp & 1);
#pragma unroll
        for (int ks = 0; ks < 4; ++ks) xp[ks] = lds + (ks >> 1) * 4096 + rh * 2048 + (ks & 1) * 1024 + lane * 16;
        static_for<0, 16>([&](auto J) {
            constexpr int j = J.value;
            constexpr int last = j == 0 ? 5 : (j + 4 < 15 ? j + 4 : 15);
            constexpr int nz = j == 12 + 1 ? 0 : 0;
            if constexpr (j > 0) WAIT_BAR((j < 3 ? 3 * (last - 2) : 3 * (last - j)) + nz + (j > 12 ? 2 : 0));
            if constexpr (j >= 1 && j + 5 <= 15) issue3(std::integral_constant<int, j + 5>{});
            if constexpr (j == 12) {
                wait_words<128>(p.ws + WS_ZF + hg * 512, TAG, lane);
#pragma unroll
                for (int i = 0; i < 2; ++i) {
                    const int row = 8 * (2 * wid + i) + (lane >> 3);
                    dma_dev(row * 4096 + (((lane & 7) ^ (((row >> 1) & 1) << 2)) * 16), p.ws + WS_ZT + (size_t)hg * 128 * 4096 + rt * 128,
                            ldsb + ((2 + 11) % 6) * SLOT + (2 * wid + i) * 1024);
                }
            }
            gemm_stage<false>(bacc, wp0, wp1, xp, ((2 + j) % 6) * SLOT);
        });
    }
    STAMP(7);
    {
        const int t = 64 * rt + 32 * rh + r32, cb = 128 * hg + 32 * hd;
        u32x2 xv[4];
#pragma unroll
        for (int i = 0; i < 4; ++i) xv[i] = *(const u32x2*)(p.x + (size_t)t * D + cb + 8 * i + 4 * h);
        const float g = __uint_as_float(ld4_dev(p.ws + WS_G + t * 4));
        WAIT_BAR(0);
        const int q = (lane & 15) >> 2, pp = lane & 3, blk = (lane >> 4) & 1;
        u32x2 ov[4];
#pragma unroll
        for (int i = 0; i < 4; ++i) {
            const int row = 32 * hd + 8 * i + 4 * h + q;
            const int chunk = 4 * rh + 2 * blk + (pp >> 1);
            const s16x4 z = ldstr(lds + ((2 + 11) % 6) * SLOT + row * 128 + 16 * (chunk ^ (((row >> 1) & 1) << 2)) + 8 * (pp & 1));
            const u32x2 zz = __builtin_bit_cast(u32x2, z);
            const float g1 = 1.f - g;
            const float o0 = fmaf(g * bflo(xv[i][0]), bflo(zz[0]), g1 * bacc[4 * i]);
            const float o1 = fmaf(g * bfhi(xv[i][0]), bfhi(zz[0]), g1 * bacc[4 * i + 1]);
            const float o2 = fmaf(g * bflo(xv[i][1]), bflo(zz[1]), g1 * bacc[4 * i + 2]);
            const float o3 = fmaf(g * bfhi(xv[i][1]), bfhi(zz[1]), g1 * bacc[4 * i + 3]);
            ov[i] = u32x2{pk2(o0, o1), pk2(o2, o3)};
        }
#pragma unroll
        for (int i = 0; i < 4; i += 2) {
            auto a = __builtin_amdgcn_permlane32_swap(ov[i][0], ov[i + 1][0], false, false);
            auto b = __builtin_amdgcn_permlane32_swap(ov[i][1], ov[i + 1][1], false, false);
            *(u32x4*)(p.out + (size_t)t * D + cb + 8 * i + 8 * h) = u32x4{a[0], b[0], a[1], b[1]};
        }
    }
    STAMP(8);
}

extern "C" void kernel_launch(void* const* d_in, const int* in_sizes, int n_in, void* d_out, int out_size, void* d_ws, size_t ws_size, hipStream_t stream) {
    Params p;
    p.x = (const u16*)d_in[0]; p.k = (const u16*)d_in[1]; p.v = (const u16*)d_in[2]; p.sk = (const u16*)d_in[3];
    p.ssmk = (const u16*)d_in[4]; p.ssmd = (const u16*)d_in[5]; p.wq = (const u16*)d_in[7]; p.wk = (const u16*)d_in[8];
    p.wv = (const u16*)d_in[9]; p.wo = (const u16*)d_in[10]; p.gw = (const u16*)d_in[11];
    p.out = (u16*)d_out; p.ws = (char*)d_ws;
    (void)hipFuncSetAttribute((const void*)fused_kernel, hipFuncAttributeMaxDynamicSharedMemorySize, LDS_BYTES);
    (void)hipMemsetAsync(d_ws, 0, WS_FLAG_BYTES, stream);
    fused_kernel<<<256, NT, LDS_BYTES, stream>>>(p);
}
```

```cpp
#define NSTG 16
#include <hip/hip_runtime.h>
#include <stdint.h>

typedef unsigned short u16;
typedef unsigned int u32;
typedef unsigned long long u64;
using bf16x8 = __attribute__((ext_vector_type(8))) short;
using s16x4 = __attribute__((ext_vector_type(4))) short;
using f32x16 = __attribute__((ext_vector_type(16))) float;
using f32x4 = __attribute__((ext_vector_type(4))) float;
using f32x2 = __attribute__((ext_vector_type(2))) float;
using bf16x2 = __attribute__((ext_vector_type(2))) __bf16;
using u32x4 = __attribute__((ext_vector_type(4))) unsigned;
using u32x2 = __attribute__((ext_vector_type(2))) unsigned;
#define LAS __attribute__((address_space(3)))
#define GAS __attribute__((address_space(1)))
typedef LAS char* lptr;

constexpr int S = 2048, D = 1024;
constexpr int NT = 512;

constexpr size_t WS_KVF = 0;
constexpr size_t WS_UF = 4096;
constexpr size_t WS_OF = 12288;
constexpr size_t WS_ZF = 20480;
constexpr size_t WS_FLAG_BYTES = 24576;
constexpr size_t WS_G = 32768;
constexpr size_t WS_KV = 65536;
constexpr size_t WS_UT = 1u << 20;
constexpr size_t WS_O = 5u << 20;
constexpr size_t WS_ZT = 9u << 20;
constexpr size_t WS_TS = 13u << 20;

constexpr int SLOT = 24576;
constexpr int L_MISC = 6 * SLOT;
constexpr int LDS_BYTES = 163840;
constexpr int L_H = 0;
constexpr int H_SEG = 136, H_CS = 1104, H_CH = 2192;
constexpr int L_U = 5 * SLOT;
constexpr int U_G = 2288, U_CH = 4576;
constexpr int L_KV = 2 * SLOT;

constexpr float QSCALE = 0.17677669529663687f * 1.4426950408889634f;
constexpr float SAFE_T2 = 64.f * 64.f;
constexpr float SAFE_V = 1099511627776.f;
constexpr u32 SPIN_CAP = 1u << 16;

struct Params {
    const u16 *x, *k, *v, *sk, *ssmk, *ssmd, *wq, *wk, *wv, *wo, *gw;
    u16* out;
    char* ws;
};

#define SBAR() __builtin_amdgcn_sched_barrier(0)
#define PIN(x) asm volatile("" : "+v"(x))
#define MFMA32(a, b, c) __builtin_amdgcn_mfma_f32_32x32x16_bf16(a, b, c, 0, 0, 0)
#define MFMA16(a, b, c) __builtin_amdgcn_mfma_f32_16x16x32_bf16(a, b, c, 0, 0, 0)
#define WAIT_BAR(N) asm volatile("s_waitcnt vmcnt(%c0) lgkmcnt(0)\n\ts_barrier" ::"i"(N) : "memory")
#define WAIT_VM0() asm volatile("s_waitcnt vmcnt(0)" ::: "memory")

#define STAMP(i) do { } while (0)

__device__ __forceinline__ u32 rfl(u32 v) { return __builtin_amdgcn_readfirstlane(v); }
__device__ __forceinline__ float bflo(u32 v) { return __uint_as_float(v << 16); }
__device__ __forceinline__ float bfhi(u32 v) { return __uint_as_float(v & 0xffff0000u); }
__device__ __forceinline__ u32 pk2(float lo, float hi) {
    f32x2 f = {lo, hi};
    return __builtin_bit_cast(u32, __builtin_convertvector(f, bf16x2));
}
__device__ __forceinline__ void dma(u32 voff, const void* sbase, u32 ldsaddr) {
    asm volatile("s_mov_b32 m0, %2\n\ts_nop 0\n\tglobal_load_lds_dwordx4 %0, %1" ::"v"(voff), "s"(sbase), "s"(ldsaddr) : "memory");
}
__device__ __forceinline__ void dma_dev(u32 voff, const void* sbase, u32 ldsaddr) {
    asm volatile("s_mov_b32 m0, %2\n\ts_nop 0\n\tglobal_load_lds_dwordx4 %0, %1 sc1" ::"v"(voff), "s"(sbase), "s"(ldsaddr) : "memory");
}
__device__ __forceinline__ void st16_dev(u32 voff, const void* sbase, u32x4 v) {
    asm volatile("global_store_dwordx4 %0, %1, %2 sc1\n\ts_nop 1" ::"v"(voff), "v"(v), "s"(sbase) : "memory");
}
__device__ __forceinline__ void st8_dev(u32 voff, const void* sbase, u32x2 v) {
    asm volatile("global_store_dwordx2 %0, %1, %2 sc1" ::"v"(voff), "v"(v), "s"(sbase) : "memory");
}
__device__ __forceinline__ void st4_dev(void* ptr, u32 v) { __hip_atomic_store((GAS u32*)ptr, v, __ATOMIC_RELAXED, __HIP_MEMORY_SCOPE_AGENT); }
__device__ __forceinline__ void st8g_dev(void* ptr, u64 v) { __hip_atomic_store((GAS u64*)ptr, v, __ATOMIC_RELAXED, __HIP_MEMORY_SCOPE_AGENT); }
__device__ __forceinline__ u32 ld4_dev(const void* ptr) { return __hip_atomic_load((GAS const u32*)ptr, __ATOMIC_RELAXED, __HIP_MEMORY_SCOPE_AGENT); }
__device__ __forceinline__ u64 ld8_dev(const void* ptr) { return __hip_atomic_load((GAS const u64*)ptr, __ATOMIC_RELAXED, __HIP_MEMORY_SCOPE_AGENT); }

__device__ __forceinline__ bf16x8 ldsb128(lptr p) { return *(const LAS bf16x8*)p; }
typedef short v4i16_t __attribute__((ext_vector_type(4)));
__device__ __forceinline__ s16x4 ldstr(lptr p) { return __builtin_bit_cast(s16x4, __builtin_amdgcn_ds_read_tr16_b64_v4i16((LAS v4i16_t*)p)); }
__device__ __forceinline__ bf16x8 cat8(s16x4 a, s16x4 b) { return (bf16x8){a[0], a[1], a[2], a[3], b[0], b[1], b[2], b[3]}; }
__device__ __forceinline__ float swap32sum(float v) {
    auto r = __builtin_amdgcn_permlane32_swap(__float_as_uint(v), __float_as_uint(v), false, false);
    return __uint_as_float(r[0]) + __uint_as_float(r[1]);
}
__device__ __forceinline__ float swap32max(float v) {
    auto r = __builtin_amdgcn_permlane32_swap(__float_as_uint(v), __float_as_uint(v), false, false);
    return fmaxf(__uint_as_float(r[0]), __uint_as_float(r[1]));
}

template <int N>
__device__ __forceinline__ void wait_words(const char* base, u32 tag, int lane) {
    for (u32 spins = 0; spins < SPIN_CAP; ++spins) {
        bool ok = true;
        if (N <= 64) ok = ld4_dev(base + (lane & (N - 1)) * 4) == tag;
        else { const u64 v = ld8_dev(base + lane * 8); ok = (u32)v == tag && (u32)(v >> 32) == tag; }
        if (__all(ok)) break;
        __builtin_amdgcn_s_sleep(1);
    }
    __builtin_amdgcn_fence(__ATOMIC_ACQUIRE, "wavefront");
}

template <bool NATURAL_K>
__device__ __forceinline__ void gemm_stage(f32x16& acc, lptr wp0, lptr wp1, const lptr (&xp)[4], int off) {
#pragma unroll
    for (int ks = 0; ks < 4; ++ks) {
        const bf16x8 a = cat8(ldstr(wp0 + off + ks * 4096), ldstr(wp1 + off + ks * 4096));
        const bf16x8 b = ldsb128(xp[ks] + off);
        acc = MFMA32(a, b, acc);
    }
}

template <int I, int N, class F>
__device__ __forceinline__ void static_for(F&& f) {
    if constexpr (I < N) { f(std::integral_constant<int, I>{}); static_for<I + 1, N>(f); }
}

__global__ __launch_bounds__(NT, 2) void fused_kernel(Params p) {
    extern __shared__ __attribute__((aligned(16))) char smem[];
    const lptr lds = (lptr)smem;
    const u32 ldsb = (u32)(size_t)smem;
    const int tid = threadIdx.x, lane = tid & 63;
    const int wid = rfl(tid >> 6);
    const int bid = blockIdx.x;
    const int xcd = bid & 7, ix = bid >> 3;
    const int rt = xcd * 4 + (ix >> 3), hg = ix & 7;
    const u32 TAG = 1u;
    const int r32 = lane & 31, h = lane >> 5;
    STAMP(0);

    const int cg = hg >> 1;
    const bool dmaw = wid >= 1 && wid <= 6;
    const int dw = wid - 1;
    u32 voA[4], voB[4];
    const char *sbA, *sbB;
    u32 stA, stB;
    if (dw < 4) {
#pragma unroll
        for (int i = 0; i < 4; ++i) { const int row = (8 * dw + 2 * i + h) & 31; voA[i] = row * 2048 + ((r32 ^ (row & 15)) * 16); }
        sbA = (const char*)p.x + (size_t)(64 * rt + 32 * (hg & 1)) * 2048; stA = 512;
    } else {
#pragma unroll
        for (int i = 0; i < 4; ++i) voA[i] = ((((dw - 4) * 4 + i) & 7) * 32 + (lane >> 1)) * 64 + (lane & 1) * 16;
        sbA = (const char*)(cg < 2 ? p.wk : p.wv) + (cg & 1) * 32; stA = 256 * 64;
    }
    if (dw < 2) {
#pragma unroll
        for (int i = 0; i < 4; ++i) { const int row = (8 * (4 * dw + i) + (lane >> 3)) & 63; voB[i] = row * 2048 + (((lane & 7) ^ ((row >> 1) & 7)) * 16); }
        sbB = (const char*)p.x + (size_t)(64 * rt) * 2048; stB = 128;
    } else {
#pragma unroll
        for (int i = 0; i < 4; ++i) { const int row = 4 * ((4 * (dw - 2) + i) & 15) + (lane >> 4); voB[i] = row * 2048 + (((lane & 15) ^ (((lane >> 4) << 2) | i)) * 16); }
        sbB = (const char*)p.wq + hg * 256; stB = 64 * 2048;
    }
    const u32 ldsw = ldsb + (dw & 7) * 4096;
    auto issue0 = [&](auto SG) {
        constexpr int sg = SG.value;
        if (dmaw) {
#pragma unroll
            for (int i = 0; i < 4; ++i) {
                if constexpr (sg < 4) dma(voA[i], sbA + (size_t)sg * stA, ldsw + (sg % 6) * SLOT + i * 1024);
                else dma(voB[i], sbB + (size_t)(sg - 4) * stB, ldsw + (sg % 6) * SLOT + i * 1024);
            }
        }
    };
    static_for<0, 6>(issue0);

    const int hd = wid >> 1, rh = wid & 1;
    lptr wp0, wp1, xp[4];
    {
        const int blk = (lane >> 4) & 1, q = (lane & 15) >> 2, pp = lane & 3;
        const int ch = 4 * hd + 2 * blk + (pp >> 1);
        wp0 = lds + 8192 + 256 * (8 * h + q) + 16 * (ch ^ ((q << 2) | (2 * h))) + 8 * (pp & 1);
        wp1 = lds + 8192 + 256 * (8 * h + 4 + q) + 16 * (ch ^ ((q << 2) | (2 * h + 1))) + 8 * (pp & 1);
        const int row = 32 * rh + r32;
#pragma unroll
        for (int ks = 0; ks < 4; ++ks) xp[ks] = lds + row * 128 + 16 * ((2 * ks + h) ^ ((row >> 1) & 7));
    }
    const bool kvw = wid == 0 || wid == 7;
    const int ku = wid == 7 ? 1 : 0;
    f32x4 kvacc = {0.f, 0.f, 0.f, 0.f};
    f32x16 qacc = {};
    float kvstat = 0.f;

    static_for<0, 20>([&](auto SG) {
        constexpr int sg = SG.value;
        constexpr int nout = sg == 0 ? 5 : (sg <= 15 ? 4 : 19 - sg);
        WAIT_BAR(4 * nout);
        if constexpr (sg >= 1 && sg + 5 <= 19) issue0(std::integral_constant<int, sg + 5>{});
        constexpr int off = (sg % 6) * SLOT;
        if constexpr (sg < 4) {
            if (kvw) {
                const int g = lane >> 4, row = 16 * ku + (lane & 15);
                const lptr xr = lds + off + row * 512;
                const lptr wr = lds + off + 16384 + (8 * g + ((lane & 15) >> 2)) * 32 + 8 * (lane & 3);
#pragma unroll
                for (int ks = 0; ks < 8; ++ks) {
                    const bf16x8 xf = ldsb128(xr + 16 * ((4 * ks + g) ^ (row & 15)));
                    const bf16x8 wf = cat8(ldstr(wr + ks * 1024), ldstr(wr + ks * 1024 + 128));
                    if (cg < 2) kvacc = MFMA16(wf, xf, kvacc); else kvacc = MFMA16(xf, wf, kvacc);
                }
                if constexpr (sg == 3) {
                    const int tile = 2 * rt + (hg & 1), c15 = lane & 15;
                    const u32 o = cg < 2 ? (u32)(cg * 1024 + ((g & 1) * 32 + 16 * ku + c15) * 16 + (g >> 1) * 8)
                                         : (u32)(2048 + ku * 1024 + ((g & 1) * 32 + 16 * (cg - 2) + c15) * 16 + (g >> 1) * 8);
                    st8_dev(o, p.ws + WS_KV + (size_t)tile * 4096, u32x2{pk2(kvacc[0], kvacc[1]), pk2(kvacc[2], kvacc[3])});
                    float m;
                    if (cg < 2) {
                        m = kvacc[0] * kvacc[0] + kvacc[1] * kvacc[1] + kvacc[2] * kvacc[2] + kvacc[3] * kvacc[3];
                        m += __shfl_xor(m, 16); m += __shfl_xor(m, 32);
                    } else {
                        m = __uint_as_float(max(max(__float_as_uint(kvacc[0]) & 0x7fffffffu, __float_as_uint(kvacc[1]) & 0x7fffffffu),
                                                max(__float_as_uint(kvacc[2]) & 0x7fffffffu, __float_as_uint(kvacc[3]) & 0x7fffffffu)));
                    }
                    u32 mb = __float_as_uint(m) & 0x7fffffffu;
#pragma unroll
                    for (int o2 = 1; o2 < 64; o2 <<= 1) mb = max(mb, (u32)__shfl_xor((int)mb, o2));
                    kvstat = __uint_as_float(mb);
                }
            }
        } else {
            gemm_stage<true>(qacc, wp0, wp1, xp, off);
            if constexpr (sg == 5) {
                if (kvw) {
                    WAIT_VM0();
                    if (lane == 0) st8g_dev(p.ws + WS_KVF + (size_t)((rt * 8 + hg) * 2 + ku) * 8, ((u64)__float_as_uint(kvstat) << 32) | TAG);
                }
            }
        }
    });
    STAMP(1);
    WAIT_BAR(0);

    bf16x8 qf0, qf1;
    float q2 = 0.f;
    {
        float qs[16];
#pragma unroll
        for (int i = 0; i < 16; ++i) { qs[i] = qacc[i] * QSCALE; q2 = fmaf(qs[i], qs[i], q2); }
        q2 = swap32sum(q2);
        qf0 = __builtin_bit_cast(bf16x8, (u32x4){pk2(qs[0], qs[1]), pk2(qs[2], qs[3]), pk2(qs[4], qs[5]), pk2(qs[6], qs[7])});
        qf1 = __builtin_bit_cast(bf16x8, (u32x4){pk2(qs[8], qs[9]), pk2(qs[10], qs[11]), pk2(qs[12], qs[13]), pk2(qs[14], qs[15])});
    }

    bool fast;
    {
        u32 k0 = 0, k1 = 0, vm = 0;
        for (u32 spins = 0; spins < SPIN_CAP; ++spins) {
            bool ok = true; k0 = 0; k1 = 0; vm = 0;
#pragma unroll
            for (int i = 0; i < 8; ++i) {
                const u64 gv = ld8_dev(p.ws + WS_KVF + (size_t)(i * 64 + lane) * 8);
                ok &= (u32)gv == TAG;
                const u32 val = (u32)(gv >> 32);
                const int c = (lane >> 2) & 3;
                k0 = max(k0, c == 0 ? val : 0u); k1 = max(k1, c == 1 ? val : 0u); vm = max(vm, c >= 2 ? val : 0u);
            }
            if (__all(ok)) break;
            __builtin_amdgcn_s_sleep(1);
        }
        __builtin_amdgcn_fence(__ATOMIC_ACQUIRE, "wavefront");
#pragma unroll
        for (int o2 = 1; o2 < 64; o2 <<= 1) {
            k0 = max(k0, (u32)__shfl_xor((int)k0, o2)); k1 = max(k1, (u32)__shfl_xor((int)k1, o2)); vm = max(vm, (u32)__shfl_xor((int)vm, o2));
        }
        const float k2 = __uint_as_float(k0) + __uint_as_float(k1);
        fast = __all(q2 * k2 <= SAFE_T2 && __uint_as_float(vm) <= SAFE_V);
    }
    const char* kvg = p.ws + WS_KV + wid * 2048;
    const u32 kvl = ldsb + L_KV + wid * 2048;
    const u32 vl16 = lane * 16;
    dma_dev(vl16, kvg, kvl); dma_dev(vl16, kvg + 1024, kvl + 1024);
    dma_dev(vl16, kvg + 16384, kvl + SLOT); dma_dev(vl16, kvg + 16384 + 1024, kvl + SLOT + 1024);
    STAMP(2);

    {
        const int cp = lane & 7, tg = lane >> 3;
        const int c0 = 128 * hg + 16 * wid + 2 * cp, t0 = 64 * rt + 8 * tg;
        u32 kr[11], vr[8];
#pragma unroll
        for (int i = 0; i < 11; ++i) { const int t = t0 - 3 + i; kr[i] = *(const u32*)(p.k + (size_t)max(t, 0) * D + c0); if (i < 3 && t < 0) kr[i] = 0u; }
#pragma unroll
        for (int i = 0; i < 8; ++i) vr[i] = *(const u32*)(p.v + (size_t)(t0 + i) * D + c0);
        const u32x4 sw = *(const u32x4*)(p.sk + c0 * 4);
        const int grow = 64 * rt + 8 * hg + wid;
        const u32x4 gx0 = *(const u32x4*)(p.x + (size_t)grow * D + lane * 8), gx1 = *(const u32x4*)(p.x + (size_t)grow * D + 512 + lane * 8);
        const u32x4 gw0 = *(const u32x4*)(p.gw + lane * 8), gw1 = *(const u32x4*)(p.gw + 512 + lane * 8);
        float u0[8], u1[8];
#pragma unroll
        for (int i = 0; i < 8; ++i) {
            const float a = fmaf(bflo(sw[0]), bflo(kr[i + 3]), fmaf(bfhi(sw[0]), bflo(kr[i + 2]), fmaf(bflo(sw[1]), bflo(kr[i + 1]), bfhi(sw[1]) * bflo(kr[i]))));
            const float b = fmaf(bflo(sw[2]), bfhi(kr[i + 3]), fmaf(bfhi(sw[2]), bfhi(kr[i + 2]), fmaf(bflo(sw[3]), bfhi(kr[i + 1]), bfhi(sw[3]) * bfhi(kr[i]))));
            u0[i] = a * bflo(vr[i]); u1[i] = b * bfhi(vr[i]);
        }
        const u32 uo = (u32)(c0 * 4096 + (2040 - t0) * 2);
        st16_dev(uo, p.ws + WS_UT, u32x4{pk2(u0[7], u0[6]), pk2(u0[5], u0[4]), pk2(u0[3], u0[2]), pk2(u0[1], u0[0])});
        st16_dev(uo + 4096, p.ws + WS_UT, u32x4{pk2(u1[7], u1[6]), pk2(u1[5], u1[4]), pk2(u1[3], u1[2]), pk2(u1[1], u1[0])});
        float gs = 0.f;
#pragma unroll
        for (int i = 0; i < 4; ++i) {
            gs = fmaf(bflo(gx0[i]), bflo(gw0[i]), gs); gs = fmaf(bfhi(gx0[i]), bfhi(gw0[i]), gs);
            gs = fmaf(bflo(gx1[i]), bflo(gw1[i]), gs); gs = fmaf(bfhi(gx1[i]), bfhi(gw1[i]), gs);
        }
#pragma unroll
        for (int o2 = 1; o2 < 64; o2 <<= 1) gs += __shfl_xor(gs, o2);
        if (lane == 0) st4_dev(p.ws + WS_G + grow * 4, __float_as_uint(1.f / (1.f + __expf(-gs))));

        const int cch = wid & 3, cpy = wid >> 2;
        const u16* hrow = p.ssmk + (size_t)(4 * bid + cch) * S;
        const lptr hb = lds + L_H + (cch * H_CH + cpy * H_CS) * 4;
#pragma unroll
        for (int i = 0; i < 5; ++i) {
            const int n = i * 64 + lane;
            if (n < 272) {
                const int seg = n / 34, cc = n - seg * 34;
                const int e = 256 * seg - 16 + 8 * cc;
                const u32x4 a = *(const u32x4*)(hrow + max(e, 0));
                const u32 prev = *(const u32*)(hrow + max(e - 2, 0));
                u32x4 w = a;
                if (cpy) w = u32x4{__builtin_amdgcn_alignbit(a[0], e >= 2 ? prev : 0u, 16), __builtin_amdgcn_alignbit(a[1], a[0], 16),
                                   __builtin_amdgcn_alignbit(a[2], a[1], 16), __builtin_amdgcn_alignbit(a[3], a[2], 16)};
                if (e < 0) w = u32x4{0u, 0u, 0u, 0u};
                *(LAS u32x4*)(hb + n * 16) = w;
            }
        }
        if (lane < 15) *(LAS u32x4*)(lds + L_U + cch * U_CH + cpy * U_G + 2048 + lane * 16) = u32x4{0u, 0u, 0u, 0u};
        WAIT_VM0();
        if (lane == 0) st4_dev(p.ws + WS_UF + ((hg * 8 + wid) * 32 + rt) * 4, TAG);
    }
    STAMP(3);

    f32x16 oacc = {};
    float linv;
    const lptr kvp = lds + L_KV + lane * 16;
    auto side = [&](int s) {
        if (s + 2 < NSTG) {
            const u32 sl = (u32)((s + 2) % 3) * SLOT;
            dma_dev(vl16, kvg + (size_t)((s + 2) & 15) * 16384, kvl + sl); dma_dev(vl16, kvg + (size_t)((s + 2) & 15) * 16384 + 1024, kvl + sl + 1024);
        }
        if (s == 12) {
            wait_words<32>(p.ws + WS_UF + (size_t)(bid >> 2) * 128, TAG, lane);
            const int cch = wid & 3, par = wid >> 2;
            const char* ug = p.ws + WS_UT + (size_t)(4 * bid + cch) * 4096 + par * 16;
            const u32 ul = ldsb + L_U + cch * U_CH + par * U_G;
            dma_dev(lane * 32, ug, ul); dma_dev(lane * 32, ug + 2048, ul + 1024);
        }
    };
    if (fast) {
        const short one = ((lane & 1) == ((lane >> 4) & 1)) ? (short)0x3f80 : (short)0;
        bf16x8 ones = {one, one, one, one, one, one, one, one};
        PIN(ones);
        f32x4 lacc = {0.f, 0.f, 0.f, 0.f};
        const f32x16 zero = {};
        WAIT_BAR(0);
        f32x16 sA, sB;
        bf16x8 k0f = ldsb128(kvp), k1f = ldsb128(kvp + 1024);
        sA = MFMA32(k0f, qf0, zero); sA = MFMA32(k1f, qf1, sA);
        k0f = ldsb128(kvp + 4096); k1f = ldsb128(kvp + 4096 + 1024);
        u32x4 pw1 = {0u, 0u, 0u, 0u};
        bf16x8 v1f = {};
        int cur = 0;
#define EX(X, i) X[i] = __builtin_amdgcn_exp2f(X[i])
#define TILE(C, N, VP, KN) do { \
        bf16x8 v0f = ldsb128((VP) + 2048); u32x4 pw0; \
        N = MFMA32(k0f, qf0, zero); EX(C, 0); EX(C, 1); pw0[0] = pk2(C[0], C[1]); EX(C, 2); PIN(C); PIN(pw0); SBAR(); \
        oacc = MFMA32(v1f, __builtin_bit_cast(bf16x8, pw1), oacc); EX(C, 3); pw0[1] = pk2(C[2], C[3]); EX(C, 4); EX(C, 5); PIN(C); PIN(pw0); SBAR(); \
        N = MFMA32(k1f, qf1, N); pw0[2] = pk2(C[4], C[5]); EX(C, 6); EX(C, 7); pw0[3] = pk2(C[6], C[7]); PIN(C); PIN(pw0); SBAR(); \
        k0f = ldsb128(KN); k1f = ldsb128((KN) + 1024); \
        lacc = MFMA16(ones, __builtin_bit_cast(bf16x8, pw1), lacc); EX(C, 8); EX(C, 9); PIN(C); SBAR(); \
        v1f = ldsb128((VP) + 3072); \
        oacc = MFMA32(v0f, __builtin_bit_cast(bf16x8, pw0), oacc); pw1[0] = pk2(C[8], C[9]); EX(C, 10); EX(C, 11); pw1[1] = pk2(C[10], C[11]); EX(C, 12); PIN(C); PIN(pw1); SBAR(); \
        lacc = MFMA16(ones, __builtin_bit_cast(bf16x8, pw0), lacc); EX(C, 13); pw1[2] = pk2(C[12], C[13]); EX(C, 14); EX(C, 15); pw1[3] = pk2(C[14], C[15]); PIN(pw1); SBAR(); \
    } while (0)
        for (int s = 0; s < NSTG; ++s) {
            side(s);
            const int nxt = cur == 2 * SLOT ? 0 : cur + SLOT;
            const lptr c = kvp + cur, n = kvp + nxt;
            SBAR();
            TILE(sA, sB, c, c + 8192);
            TILE(sB, sA, c + 4096, c + 12288);
            TILE(sA, sB, c + 8192, n);
            TILE(sB, sA, c + 12288, n + 4096);
            cur = nxt;
            WAIT_BAR(0);
        }
        oacc = MFMA32(v1f, __builtin_bit_cast(bf16x8, pw1), oacc);
        lacc = MFMA16(ones, __builtin_bit_cast(bf16x8, pw1), lacc);
        linv = 1.f / ((lane & 16) ? lacc[1] : lacc[0]);
#undef TILE
#undef EX
    } else {
        float m = -__builtin_inff(), l = 0.f;
        const f32x16 zero = {};
        int cur = 0;
        WAIT_BAR(0);
        for (int s = 0; s < NSTG; ++s) {
            side(s);
            for (int t = 0; t < 4; ++t) {
                const lptr c = kvp + cur + t * 4096;
                f32x16 sc = MFMA32(ldsb128(c), qf0, zero);
                sc = MFMA32(ldsb128(c + 1024), qf1, sc);
                float mx = sc[0];
#pragma unroll
                for (int i = 1; i < 16; ++i) mx = fmaxf(mx, sc[i]);
                const float mn = fmaxf(m, swap32max(mx));
                const float al = __builtin_amdgcn_exp2f(m - mn);
                m = mn;
                float ps = 0.f;
#pragma unroll
                for (int i = 0; i < 16; ++i) { sc[i] = __builtin_amdgcn_exp2f(sc[i] - mn); ps += sc[i]; oacc[i] *= al; }
                l = l * al + ps;
                const u32x4 a = {pk2(sc[0], sc[1]), pk2(sc[2], sc[3]), pk2(sc[4], sc[5]), pk2(sc[6], sc[7])};
                const u32x4 b = {pk2(sc[8], sc[9]), pk2(sc[10], sc[11]), pk2(sc[12], sc[13]), pk2(sc[14], sc[15])};
                oacc = MFMA32(ldsb128(c + 2048), __builtin_bit_cast(bf16x8, a), oacc);
                oacc = MFMA32(ldsb128(c + 3072), __builtin_bit_cast(bf16x8, b), oacc);
            }
            cur = cur == 2 * SLOT ? 0 : cur + SLOT;
            WAIT_BAR(0);
        }
        linv = 1.f / swap32sum(l);
    }
    STAMP(4);
    {
        const char* og = p.ws + WS_O + (size_t)((rt * 32 + hg * 4 + hd) * 2 + rh) * 2048;
        st16_dev(vl16, og, u32x4{pk2(oacc[0] * linv, oacc[1] * linv), pk2(oacc[2] * linv, oacc[3] * linv), pk2(oacc[4] * linv, oacc[5] * linv), pk2(oacc[6] * linv, oacc[7] * linv)});
        st16_dev(vl16, og + 1024, u32x4{pk2(oacc[8] * linv, oacc[9] * linv), pk2(oacc[10] * linv, oacc[11] * linv), pk2(oacc[12] * linv, oacc[13] * linv), pk2(oacc[14] * linv, oacc[15] * linv)});
        WAIT_VM0();
        if (lane == 0) st4_dev(p.ws + WS_OF + (rt * 64 + hg * 8 + wid) * 4, TAG);
    }
    STAMP(5);

    u32 voC[3];
    const char* sbC[3];
    u32 stC[3];
#pragma unroll
    for (int i = 0; i < 3; ++i) {
        const int pc = wid * 3 + i;
        if (pc < 8) { voC[i] = vl16; sbC[i] = p.ws + WS_O + (size_t)rt * 131072 + pc * 1024; stC[i] = 8192; }
        else {
            const int q = pc - 8;
            voC[i] = (4 * q + (lane >> 4)) * 2048 + (((lane & 15) ^ (((lane >> 4) << 2) | (q & 3))) * 16);
            sbC[i] = (const char*)p.wo + hg * 256; stC[i] = 64 * 2048;
        }
    }
    if (wid >= 4) {
        const int hw = wid - 4;
#pragma unroll
        for (int j = 0; j < 3; ++j)
#pragma unroll
            for (int i = 0; i < 4; ++i) {
                const int q = 4 * hw + i;
                dma((4 * q + (lane >> 4)) * 2048 + (((lane & 15) ^ (((lane >> 4) << 2) | i)) * 16), (const char*)p.wo + hg * 256 + (size_t)j * 64 * 2048,
                    ldsb + (2 + j) * SLOT + 8192 + q * 1024);
            }
        wait_words<64>(p.ws + WS_OF + rt * 256, TAG, lane);
#pragma unroll
        for (int j = 0; j < 3; ++j)
#pragma unroll
            for (int i = 0; i < 2; ++i)
                dma_dev(vl16, p.ws + WS_O + (size_t)rt * 131072 + j * 8192 + (2 * hw + i) * 1024, ldsb + (2 + j) * SLOT + (2 * hw + i) * 1024);
    } else {
        const int al = (lane >> 4) & 1, m = lane & 15;
        const lptr ap = lds + L_H + (wid * H_CH + ((m & 1) ? 0 : H_CS) + al * H_SEG + ((m + 2) >> 1) + 4 * h) * 4;
        const lptr bp = lds + L_U + wid * U_CH + h * U_G + 16 * (127 - 16 * al - m);
        f32x16 c0 = {}, c1 = {}, c2 = {}, c3 = {};
#pragma unroll 2
        for (int kp = 0; kp < 16; ++kp) {
            bf16x8 a[4], b[4];
#pragma unroll
            for (int i = 0; i < 4; ++i) {
                const LAS u32* q = (const LAS u32*)(ap + (2 * i * H_SEG + 8 * kp) * 4);
                a[i] = __builtin_bit_cast(bf16x8, (u32x4){q[0], q[1], q[2], q[3]});
                b[i] = ldsb128(bp + 16 * kp - 512 * i);
            }
            c3 = MFMA32(a[0], b[3], c3); c2 = MFMA32(a[0], b[2], c2); c1 = MFMA32(a[0], b[1], c1); c0 = MFMA32(a[0], b[0], c0);
            c3 = MFMA32(a[1], b[2], c3); c2 = MFMA32(a[1], b[1], c2); c1 = MFMA32(a[1], b[0], c1);
            c3 = MFMA32(a[2], b[1], c3); c2 = MFMA32(a[2], b[0], c2);
            c3 = MFMA32(a[3], b[0], c3);
        }
        float y[8][4];
        auto fold = [&](const f32x16& c, int C) {
#pragma unroll
            for (int i = 0; i < 4; ++i) {
                auto x = __builtin_amdgcn_permlane16_swap(__float_as_uint(c[i]), __float_as_uint(c[i + 4]), false, false);
                auto r = __builtin_amdgcn_permlane16_swap(__float_as_uint(c[8 + i]), __float_as_uint(c[12 + i]), false, false);
                if (C == 0) y[0][i] = __uint_as_float(x[0]); else y[2 * C][i] += __uint_as_float(x[0]);
                y[2 * C + 1][i] = __uint_as_float(x[1]) + __uint_as_float(r[0]);
                if (C < 3) y[2 * C + 2][i] = __uint_as_float(r[1]);
            }
        };
        fold(c0, 0); fold(c1, 1); fold(c2, 2); fold(c3, 3);
        const int ch = 4 * bid + wid;
        const float dd = __uint_as_float((u32)p.ssmd[ch] << 16);
        const int tl = 16 * m + 8 * al + 4 * h;
        const char* zg = p.ws + WS_ZT + (size_t)ch * 4096;
#pragma unroll
        for (int c = 0; c < 8; ++c) {
            const int xr = 2044 - 256 * c - tl;
            const u32x2 uu = *(const LAS u32x2*)(lds + L_U + wid * U_CH + ((xr >> 3) & 1) * U_G + (xr >> 4) * 16 + (xr & 4) * 2);
            const float z0 = fmaf(bfhi(uu[1]), dd, y[c][0]), z1 = fmaf(bflo(uu[1]), dd, y[c][1]);
            const float z2 = fmaf(bfhi(uu[0]), dd, y[c][2]), z3 = fmaf(bflo(uu[0]), dd, y[c][3]);
            st8_dev((u32)(256 * c + tl) * 2, zg, u32x2{pk2(z0, z1), pk2(z2, z3)});
        }
        WAIT_VM0();
        if (lane == 0) st4_dev(p.ws + WS_ZF + ch * 4, TAG);
    }
    STAMP(6);

    f32x16 bacc = {};
    {
        WAIT_BAR(0);
        auto issue3 = [&](auto J) {
            constexpr int j = J.value;
#pragma unroll
            for (int i = 0; i < 3; ++i) {
                const u32 dst = ldsb + ((2 + j) % 6) * SLOT + (wid * 3 + i) * 1024;
                if (wid * 3 + i < 8) dma_dev(voC[i], sbC[i] + (size_t)j * stC[i], dst); else dma(voC[i], sbC[i] + (size_t)j * stC[i], dst);
            }
        };
        static_for<3, 6>(issue3);
        const int blk = (lane >> 4) & 1, q = (lane & 15) >> 2, pp = lane & 3;
        const int ch = 4 * hd + 2 * blk + (pp >> 1);
        wp0 = lds + 8192 + 256 * (4 * h + q) + 16 * (ch ^ ((q << 2) | h)) + 8 * (pp & 1);
        wp1 = lds + 8192 + 256 * (8 + 4 * h + q) + 16 * (ch ^ ((q << 2) | (2 + h))) + 8 * (pp & 1);
#pragma unroll
        for (int ks = 0; ks < 4; ++ks) xp[ks] = lds + (ks >> 1) * 4096 + rh * 2048 + (ks & 1) * 1024 + lane * 16;
        static_for<0, 16>([&](auto J) {
            constexpr int j = J.value;
            constexpr int last = j == 0 ? 5 : (j + 4 < 15 ? j + 4 : 15);
            constexpr int nz = j == 12 + 1 ? 0 : 0;
            if constexpr (j > 0) WAIT_BAR((j < 3 ? 3 * (last - 2) : 3 * (last - j)) + nz + (j > 12 ? 2 : 0));
            if constexpr (j >= 1 && j + 5 <= 15) issue3(std::integral_constant<int, j + 5>{});
            if constexpr (j == 12) {
                wait_words<128>(p.ws + WS_ZF + hg * 512, TAG, lane);
#pragma unroll
                for (int i = 0; i < 2; ++i) {
                    const int row = 8 * (2 * wid + i) + (lane >> 3);
                    dma_dev(row * 4096 + (((lane & 7) ^ (((row >> 1) & 1) << 2)) * 16), p.ws + WS_ZT + (size_t)hg * 128 * 4096 + rt * 128,
                            ldsb + ((2 + 11) % 6) * SLOT + (2 * wid + i) * 1024);
                }
            }
            gemm_stage<false>(bacc, wp0, wp1, xp, ((2 + j) % 6) * SLOT);
        });
    }
    STAMP(7);
    {
        const int t = 64 * rt + 32 * rh + r32, cb = 128 * hg + 32 * hd;
        u32x2 xv[4];
#pragma unroll
        for (int i = 0; i < 4; ++i) xv[i] = *(const u32x2*)(p.x + (size_t)t * D + cb + 8 * i + 4 * h);
        const float g = __uint_as_float(ld4_dev(p.ws + WS_G + t * 4));
        WAIT_BAR(0);
        const int q = (lane & 15) >> 2, pp = lane & 3, blk = (lane >> 4) & 1;
        u32x2 ov[4];
#pragma unroll
        for (int i = 0; i < 4; ++i) {
            const int row = 32 * hd + 8 * i + 4 * h + q;
            const int chunk = 4 * rh + 2 * blk + (pp >> 1);
            const s16x4 z = ldstr(lds + ((2 + 11) % 6) * SLOT + row * 128 + 16 * (chunk ^ (((row >> 1) & 1) << 2)) + 8 * (pp & 1));
            const u32x2 zz = __builtin_bit_cast(u32x2, z);
            const float g1 = 1.f - g;
            const float o0 = fmaf(g * bflo(xv[i][0]), bflo(zz[0]), g1 * bacc[4 * i]);
            const float o1 = fmaf(g * bfhi(xv[i][0]), bfhi(zz[0]), g1 * bacc[4 * i + 1]);
            const float o2 = fmaf(g * bflo(xv[i][1]), bflo(zz[1]), g1 * bacc[4 * i + 2]);
            const float o3 = fmaf(g * bfhi(xv[i][1]), bfhi(zz[1]), g1 * bacc[4 * i + 3]);
            ov[i] = u32x2{pk2(o0, o1), pk2(o2, o3)};
        }
#pragma unroll
        for (int i = 0; i < 4; i += 2) {
            auto a = __builtin_amdgcn_permlane32_swap(ov[i][0], ov[i + 1][0], false, false);
            auto b = __builtin_amdgcn_permlane32_swap(ov[i][1], ov[i + 1][1], false, false);
            *(u32x4*)(p.out + (size_t)t * D + cb + 8 * i + 8 * h) = u32x4{a[0], b[0], a[1], b[1]};
        }
    }
    STAMP(8);
}

extern "C" void kernel_launch(void* const* d_in, const int* in_sizes, int n_in, void* d_out, int out_size, void* d_ws, size_t ws_size, hipStream_t stream) {
    Params p;
    p.x = (const u16*)d_in[0]; p.k = (const u16*)d_in[1]; p.v = (const u16*)d_in[2]; p.sk = (const u16*)d_in[3];
    p.ssmk = (const u16*)d_in[4]; p.ssmd = (const u16*)d_in[5]; p.wq = (const u16*)d_in[7]; p.wk = (const u16*)d_in[8];
    p.wv = (const u16*)d_in[9]; p.wo = (const u16*)d_in[10]; p.gw = (const u16*)d_in[11];
    p.out = (u16*)d_out; p.ws = (char*)d_ws;
    (void)hipFuncSetAttribute((const void*)fused_kernel, hipFuncAttributeMaxDynamicSharedMemorySize, LDS_BYTES);
    (void)hipMemsetAsync(d_ws, 0, WS_FLAG_BYTES, stream);
    fused_kernel<<<256, NT, LDS_BYTES, stream>>>(p);
}
```

```cpp
#define PADAT 8
#include <hip/hip_runtime.h>
#include <stdint.h>

typedef unsigned short u16;
typedef unsigned int u32;
typedef unsigned long long u64;
using bf16x8 = __attribute__((ext_vector_type(8))) short;
using s16x4 = __attribute__((ext_vector_type(4))) short;
using f32x16 = __attribute__((ext_vector_type(16))) float;
using f32x4 = __attribute__((ext_vector_type(4))) float;
using f32x2 = __attribute__((ext_vector_type(2))) float;
using bf16x2 = __attribute__((ext_vector_type(2))) __bf16;
using u32x4 = __attribute__((ext_vector_type(4))) unsigned;
using u32x2 = __attribute__((ext_vector_type(2))) unsigned;
#define LAS __attribute__((address_space(3)))
#define GAS __attribute__((address_space(1)))
typedef LAS char* lptr;

constexpr int S = 2048, D = 1024;
constexpr int NT = 512;

constexpr size_t WS_KVF = 0;
constexpr size_t WS_UF = 4096;
constexpr size_t WS_OF = 12288;
constexpr size_t WS_ZF = 20480;
constexpr size_t WS_FLAG_BYTES = 24576;
constexpr size_t WS_G = 32768;
constexpr size_t WS_KV = 65536;
constexpr size_t WS_UT = 1u << 20;
constexpr size_t WS_O = 5u << 20;
constexpr size_t WS_ZT = 9u << 20;
constexpr size_t WS_TS = 13u << 20;

constexpr int SLOT = 24576;
constexpr int L_MISC = 6 * SLOT;
constexpr int LDS_BYTES = 163840;
constexpr int L_H = 0;
constexpr int H_SEG = 136, H_CS = 1104, H_CH = 2192;
constexpr int L_U = 5 * SLOT;
constexpr int U_G = 2288, U_CH = 4576;
constexpr int L_KV = 2 * SLOT;

constexpr float QSCALE = 0.17677669529663687f * 1.4426950408889634f;
constexpr float SAFE_T2 = 64.f * 64.f;
constexpr float SAFE_V = 1099511627776.f;
constexpr u32 SPIN_CAP = 1u << 16;

struct Params {
    const u16 *x, *k, *v, *sk, *ssmk, *ssmd, *wq, *wk, *wv, *wo, *gw;
    u16* out;
    char* ws;
};

#define SBAR() __builtin_amdgcn_sched_barrier(0)
#define PIN(x) asm volatile("" : "+v"(x))
#define MFMA32(a, b, c) __builtin_amdgcn_mfma_f32_32x32x16_bf16(a, b, c, 0, 0, 0)
#define MFMA16(a, b, c) __builtin_amdgcn_mfma_f32_16x16x32_bf16(a, b, c, 0, 0, 0)
#define WAIT_BAR(N) asm volatile("s_waitcnt vmcnt(%c0) lgkmcnt(0)\n\ts_barrier" ::"i"(N) : "memory")
#define WAIT_VM0() asm volatile("s_waitcnt vmcnt(0)" ::: "memory")

#define STAMP(i) do { if ((i) == PADAT) { while (__builtin_amdgcn_s_memrealtime() - tstart_ < 7000ull) __builtin_amdgcn_s_sleep(1); } } while (0)

__device__ __forceinline__ u32 rfl(u32 v) { return __builtin_amdgcn_readfirstlane(v); }
__device__ __forceinline__ float bflo(u32 v) { return __uint_as_float(v << 16); }
__device__ __forceinline__ float bfhi(u32 v) { return __uint_as_float(v & 0xffff0000u); }
__device__ __forceinline__ u32 pk2(float lo, float hi) {
    f32x2 f = {lo, hi};
    return __builtin_bit_cast(u32, __builtin_convertvector(f, bf16x2));
}
__device__ __forceinline__ void dma(u32 voff, const void* sbase, u32 ldsaddr) {
    asm volatile("s_mov_b32 m0, %2\n\ts_nop 0\n\tglobal_load_lds_dwordx4 %0, %1" ::"v"(voff), "s"(sbase), "s"(ldsaddr) : "memory");
}
__device__ __forceinline__ void dma_dev(u32 voff, const void* sbase, u32 ldsaddr) {
    asm volatile("s_mov_b32 m0, %2\n\ts_nop 0\n\tglobal_load_lds_dwordx4 %0, %1 sc1" ::"v"(voff), "s"(sbase), "s"(ldsaddr) : "memory");
}
__device__ __forceinline__ void st16_dev(u32 voff, const void* sbase, u32x4 v) {
    asm volatile("global_store_dwordx4 %0, %1, %2 sc1\n\ts_nop 1" ::"v"(voff), "v"(v), "s"(sbase) : "memory");
}
__device__ __forceinline__ void st8_dev(u32 voff, const void* sbase, u32x2 v) {
    asm volatile("global_store_dwordx2 %0, %1, %2 sc1" ::"v"(voff), "v"(v), "s"(sbase) : "memory");
}
__device__ __forceinline__ void st4_dev(void* ptr, u32 v) { __hip_atomic_store((GAS u32*)ptr, v, __ATOMIC_RELAXED, __HIP_MEMORY_SCOPE_AGENT); }
__device__ __forceinline__ void st8g_dev(void* ptr, u64 v) { __hip_atomic_store((GAS u64*)ptr, v, __ATOMIC_RELAXED, __HIP_MEMORY_SCOPE_AGENT); }
__device__ __forceinline__ u32 ld4_dev(const void* ptr) { return __hip_atomic_load((GAS const u32*)ptr, __ATOMIC_RELAXED, __HIP_MEMORY_SCOPE_AGENT); }
__device__ __forceinline__ u64 ld8_dev(const void* ptr) { return __hip_atomic_load((GAS const u64*)ptr, __ATOMIC_RELAXED, __HIP_MEMORY_SCOPE_AGENT); }

__device__ __forceinline__ bf16x8 ldsb128(lptr p) { return *(const LAS bf16x8*)p; }
typedef short v4i16_t __attribute__((ext_vector_type(4)));
__device__ __forceinline__ s16x4 ldstr(lptr p) { return __builtin_bit_cast(s16x4, __builtin_amdgcn_ds_read_tr16_b64_v4i16((LAS v4i16_t*)p)); }
__device__ __forceinline__ bf16x8 cat8(s16x4 a, s16x4 b) { return (bf16x8){a[0], a[1], a[2], a[3], b[0], b[1], b[2], b[3]}; }
__device__ __forceinline__ float swap32sum(float v) {
    auto r = __builtin_amdgcn_permlane32_swap(__float_as_uint(v), __float_as_uint(v), false, false);
    return __uint_as_float(r[0]) + __uint_as_float(r[1]);
}
__device__ __forceinline__ float swap32max(float v) {
    auto r = __builtin_amdgcn_permlane32_swap(__float_as_uint(v), __float_as_uint(v), false, false);
    return fmaxf(__uint_as_float(r[0]), __uint_as_float(r[1]));
}

template <int N>
__device__ __forceinline__ void wait_words(const char* base, u32 tag, int lane) {
    for (u32 spins = 0; spins < SPIN_CAP; ++spins) {
        bool ok = true;
        if (N <= 64) ok = ld4_dev(base + (lane & (N - 1)) * 4) == tag;
        else { const u64 v = ld8_dev(base + lane * 8); ok = (u32)v == tag && (u32)(v >> 32) == tag; }
        if (__all(ok)) break;
        __builtin_amdgcn_s_sleep(1);
    }
    __builtin_amdgcn_fence(__ATOMIC_ACQUIRE, "wavefront");
}

template <bool NATURAL_K>
__device__ __forceinline__ void gemm_stage(f32x16& acc, lptr wp0, lptr wp1, const lptr (&xp)[4], int off) {
#pragma unroll
    for (int ks = 0; ks < 4; ++ks) {
        const bf16x8 a = cat8(ldstr(wp0 + off + ks * 4096), ldstr(wp1 + off + ks * 4096));
        const bf16x8 b = ldsb128(xp[ks] + off);
        acc = MFMA32(a, b, acc);
    }
}

template <int I, int N, class F>
__device__ __forceinline__ void static_for(F&& f) {
    if constexpr (I < N) { f(std::integral_constant<int, I>{}); static_for<I + 1, N>(f); }
}

__global__ __launch_bounds__(NT, 2) void fused_kernel(Params p) {
    extern __shared__ __attribute__((aligned(16))) char smem[];
    const lptr lds = (lptr)smem;
    const u32 ldsb = (u32)(size_t)smem;
    const int tid = threadIdx.x, lane = tid & 63;
    const int wid = rfl(tid >> 6);
    const int bid = blockIdx.x;
    const int xcd = bid & 7, ix = bid >> 3;
    const int rt = xcd * 4 + (ix >> 3), hg = ix & 7;
    const u32 TAG = 1u;
    const int r32 = lane & 31, h = lane >> 5;
    const u64 tstart_ = __builtin_amdgcn_s_memrealtime();
    STAMP(0);

    const int cg = hg >> 1;
    const bool dmaw = wid >= 1 && wid <= 6;
    const int dw = wid - 1;
    u32 voA[4], voB[4];
    const char *sbA, *sbB;
    u32 stA, stB;
    if (dw < 4) {
#pragma unroll
        for (int i = 0; i < 4; ++i) { const int row = (8 * dw + 2 * i + h) & 31; voA[i] = row * 2048 + ((r32 ^ (row & 15)) * 16); }
        sbA = (const char*)p.x + (size_t)(64 * rt + 32 * (hg & 1)) * 2048; stA = 512;
    } else {
#pragma unroll
        for (int i = 0; i < 4; ++i) voA[i] = ((((dw - 4) * 4 + i) & 7) * 32 + (lane >> 1)) * 64 + (lane & 1) * 16;
        sbA = (const char*)(cg < 2 ? p.wk : p.wv) + (cg & 1) * 32; stA = 256 * 64;
    }
    if (dw < 2) {
#pragma unroll
        for (int i = 0; i < 4; ++i) { const int row = (8 * (4 * dw + i) + (lane >> 3)) & 63; voB[i] = row * 2048 + (((lane & 7) ^ ((row >> 1) & 7)) * 16); }
        sbB = (const char*)p.x + (size_t)(64 * rt) * 2048; stB = 128;
    } else {
#pragma unroll
        for (int i = 0; i < 4; ++i) { const int row = 4 * ((4 * (dw - 2) + i) & 15) + (lane >> 4); voB[i] = row * 2048 + (((lane & 15) ^ (((lane >> 4) << 2) | i)) * 16); }
        sbB = (const char*)p.wq + hg * 256; stB = 64 * 2048;
    }
    const u32 ldsw = ldsb + (dw & 7) * 4096;
    auto issue0 = [&](auto SG) {
        constexpr int sg = SG.value;
        if (dmaw) {
#pragma unroll
            for (int i = 0; i < 4; ++i) {
                if constexpr (sg < 4) dma(voA[i], sbA + (size_t)sg * stA, ldsw + (sg % 6) * SLOT + i * 1024);
                else dma(voB[i], sbB + (size_t)(sg - 4) * stB, ldsw + (sg % 6) * SLOT + i * 1024);
            }
        }
    };
    static_for<0, 6>(issue0);

    const int hd = wid >> 1, rh = wid & 1;
    lptr wp0, wp1, xp[4];
    {
        const int blk = (lane >> 4) & 1, q = (lane & 15) >> 2, pp = lane & 3;
        const int ch = 4 * hd + 2 * blk + (pp >> 1);
        wp0 = lds + 8192 + 256 * (8 * h + q) + 16 * (ch ^ ((q << 2) | (2 * h))) + 8 * (pp & 1);
        wp1 = lds + 8192 + 256 * (8 * h + 4 + q) + 16 * (ch ^ ((q << 2) | (2 * h + 1))) + 8 * (pp & 1);
        const int row = 32 * rh + r32;
#pragma unroll
        for (int ks = 0; ks < 4; ++ks) xp[ks] = lds + row * 128 + 16 * ((2 * ks + h) ^ ((row >> 1) & 7));
    }
    const bool kvw = wid == 0 || wid == 7;
    const int ku = wid == 7 ? 1 : 0;
    f32x4 kvacc = {0.f, 0.f, 0.f, 0.f};
    f32x16 qacc = {};
    float kvstat = 0.f;

    static_for<0, 20>([&](auto SG) {
        constexpr int sg = SG.value;
        constexpr int nout = sg == 0 ? 5 : (sg <= 15 ? 4 : 19 - sg);
        WAIT_BAR(4 * nout);
        if constexpr (sg >= 1 && sg + 5 <= 19) issue0(std::integral_constant<int, sg + 5>{});
        constexpr int off = (sg % 6) * SLOT;
        if constexpr (sg < 4) {
            if (kvw) {
                const int g = lane >> 4, row = 16 * ku + (lane & 15);
                const lptr xr = lds + off + row * 512;
                const lptr wr = lds + off + 16384 + (8 * g + ((lane & 15) >> 2)) * 32 + 8 * (lane & 3);
#pragma unroll
                for (int ks = 0; ks < 8; ++ks) {
                    const bf16x8 xf = ldsb128(xr + 16 * ((4 * ks + g) ^ (row & 15)));
                    const bf16x8 wf = cat8(ldstr(wr + ks * 1024), ldstr(wr + ks * 1024 + 128));
                    if (cg < 2) kvacc = MFMA16(wf, xf, kvacc); else kvacc = MFMA16(xf, wf, kvacc);
                }
                if constexpr (sg == 3) {
                    const int tile = 2 * rt + (hg & 1), c15 = lane & 15;
                    const u32 o = cg < 2 ? (u32)(cg * 1024 + ((g & 1) * 32 + 16 * ku + c15) * 16 + (g >> 1) * 8)
                                         : (u32)(2048 + ku * 1024 + ((g & 1) * 32 + 16 * (cg - 2) + c15) * 16 + (g >> 1) * 8);
                    st8_dev(o, p.ws + WS_KV + (size_t)tile * 4096, u32x2{pk2(kvacc[0], kvacc[1]), pk2(kvacc[2], kvacc[3])});
                    float m;
                    if (cg < 2) {
                        m = kvacc[0] * kvacc[0] + kvacc[1] * kvacc[1] + kvacc[2] * kvacc[2] + kvacc[3] * kvacc[3];
                        m += __shfl_xor(m, 16); m += __shfl_xor(m, 32);
                    } else {
                        m = __uint_as_float(max(max(__float_as_uint(kvacc[0]) & 0x7fffffffu, __float_as_uint(kvacc[1]) & 0x7fffffffu),
                                                max(__float_as_uint(kvacc[2]) & 0x7fffffffu, __float_as_uint(kvacc[3]) & 0x7fffffffu)));
                    }
                    u32 mb = __float_as_uint(m) & 0x7fffffffu;
#pragma unroll
                    for (int o2 = 1; o2 < 64; o2 <<= 1) mb = max(mb, (u32)__shfl_xor((int)mb, o2));
                    kvstat = __uint_as_float(mb);
                }
            }
        } else {
            gemm_stage<true>(qacc, wp0, wp1, xp, off);
            if constexpr (sg == 5) {
                if (kvw) {
                    WAIT_VM0();
                    if (lane == 0) st8g_dev(p.ws + WS_KVF + (size_t)((rt * 8 + hg) * 2 + ku) * 8, ((u64)__float_as_uint(kvstat) << 32) | TAG);
                }
            }
        }
    });
    STAMP(1);
    WAIT_BAR(0);

    bf16x8 qf0, qf1;
    float q2 = 0.f;
    {
        float qs[16];
#pragma unroll
        for (int i = 0; i < 16; ++i) { qs[i] = qacc[i] * QSCALE; q2 = fmaf(qs[i], qs[i], q2); }
        q2 = swap32sum(q2);
        qf0 = __builtin_bit_cast(bf16x8, (u32x4){pk2(qs[0], qs[1]), pk2(qs[2], qs[3]), pk2(qs[4], qs[5]), pk2(qs[6], qs[7])});
        qf1 = __builtin_bit_cast(bf16x8, (u32x4){pk2(qs[8], qs[9]), pk2(qs[10], qs[11]), pk2(qs[12], qs[13]), pk2(qs[14], qs[15])});
    }

    bool fast;
    {
        u32 k0 = 0, k1 = 0, vm = 0;
        for (u32 spins = 0; spins < SPIN_CAP; ++spins) {
            bool ok = true; k0 = 0; k1 = 0; vm = 0;
#pragma unroll
            for (int i = 0; i < 8; ++i) {
                const u64 gv = ld8_dev(p.ws + WS_KVF + (size_t)(i * 64 + lane) * 8);
                ok &= (u32)gv == TAG;
                const u32 val = (u32)(gv >> 32);
                const int c = (lane >> 2) & 3;
                k0 = max(k0, c == 0 ? val : 0u); k1 = max(k1, c == 1 ? val : 0u); vm = max(vm, c >= 2 ? val : 0u);
            }
            if (__all(ok)) break;
            __builtin_amdgcn_s_sleep(1);
        }
        __builtin_amdgcn_fence(__ATOMIC_ACQUIRE, "wavefront");
#pragma unroll
        for (int o2 = 1; o2 < 64; o2 <<= 1) {
            k0 = max(k0, (u32)__shfl_xor((int)k0, o2)); k1 = max(k1, (u32)__shfl_xor((int)k1, o2)); vm = max(vm, (u32)__shfl_xor((int)vm, o2));
        }
        const float k2 = __uint_as_float(k0) + __uint_as_float(k1);
        fast = __all(q2 * k2 <= SAFE_T2 && __uint_as_float(vm) <= SAFE_V);
    }
    const char* kvg = p.ws + WS_KV + wid * 2048;
    const u32 kvl = ldsb + L_KV + wid * 2048;
    const u32 vl16 = lane * 16;
    dma_dev(vl16, kvg, kvl); dma_dev(vl16, kvg + 1024, kvl + 1024);
    dma_dev(vl16, kvg + 16384, kvl + SLOT); dma_dev(vl16, kvg + 16384 + 1024, kvl + SLOT + 1024);
    STAMP(2);

    {
        const int cp = lane & 7, tg = lane >> 3;
        const int c0 = 128 * hg + 16 * wid + 2 * cp, t0 = 64 * rt + 8 * tg;
        u32 kr[11], vr[8];
#pragma unroll
        for (int i = 0; i < 11; ++i) { const int t = t0 - 3 + i; kr[i] = *(const u32*)(p.k + (size_t)max(t, 0) * D + c0); if (i < 3 && t < 0) kr[i] = 0u; }
#pragma unroll
        for (int i = 0; i < 8; ++i) vr[i] = *(const u32*)(p.v + (size_t)(t0 + i) * D + c0);
        const u32x4 sw = *(const u32x4*)(p.sk + c0 * 4);
        const int grow = 64 * rt + 8 * hg + wid;
        const u32x4 gx0 = *(const u32x4*)(p.x + (size_t)grow * D + lane * 8), gx1 = *(const u32x4*)(p.x + (size_t)grow * D + 512 + lane * 8);
        const u32x4 gw0 = *(const u32x4*)(p.gw + lane * 8), gw1 = *(const u32x4*)(p.gw + 512 + lane * 8);
        float u0[8], u1[8];
#pragma unroll
        for (int i = 0; i < 8; ++i) {
            const float a = fmaf(bflo(sw[0]), bflo(kr[i + 3]), fmaf(bfhi(sw[0]), bflo(kr[i + 2]), fmaf(bflo(sw[1]), bflo(kr[i + 1]), bfhi(sw[1]) * bflo(kr[i]))));
            const float b = fmaf(bflo(sw[2]), bfhi(kr[i + 3]), fmaf(bfhi(sw[2]), bfhi(kr[i + 2]), fmaf(bflo(sw[3]), bfhi(kr[i + 1]), bfhi(sw[3]) * bfhi(kr[i]))));
            u0[i] = a * bflo(vr[i]); u1[i] = b * bfhi(vr[i]);
        }
        const u32 uo = (u32)(c0 * 4096 + (2040 - t0) * 2);
        st16_dev(uo, p.ws + WS_UT, u32x4{pk2(u0[7], u0[6]), pk2(u0[5], u0[4]), pk2(u0[3], u0[2]), pk2(u0[1], u0[0])});
        st16_dev(uo + 4096, p.ws + WS_UT, u32x4{pk2(u1[7], u1[6]), pk2(u1[5], u1[4]), pk2(u1[3], u1[2]), pk2(u1[1], u1[0])});
        float gs = 0.f;
#pragma unroll
        for (int i = 0; i < 4; ++i) {
            gs = fmaf(bflo(gx0[i]), bflo(gw0[i]), gs); gs = fmaf(bfhi(gx0[i]), bfhi(gw0[i]), gs);
            gs = fmaf(bflo(gx1[i]), bflo(gw1[i]), gs); gs = fmaf(bfhi(gx1[i]), bfhi(gw1[i]), gs);
        }
#pragma unroll
        for (int o2 = 1; o2 < 64; o2 <<= 1) gs += __shfl_xor(gs, o2);
        if (lane == 0) st4_dev(p.ws + WS_G + grow * 4, __float_as_uint(1.f / (1.f + __expf(-gs))));

        const int cch = wid & 3, cpy = wid >> 2;
        const u16* hrow = p.ssmk + (size_t)(4 * bid + cch) * S;
        const lptr hb = lds + L_H + (cch * H_CH + cpy * H_CS) * 4;
#pragma unroll
        for (int i = 0; i < 5; ++i) {
            const int n = i * 64 + lane;
            if (n < 272) {
                const int seg = n / 34, cc = n - seg * 34;
                const int e = 256 * seg - 16 + 8 * cc;
                const u32x4 a = *(const u32x4*)(hrow + max(e, 0));
                const u32 prev = *(const u32*)(hrow + max(e - 2, 0));
                u32x4 w = a;
                if (cpy) w = u32x4{__builtin_amdgcn_alignbit(a[0], e >= 2 ? prev : 0u, 16), __builtin_amdgcn_alignbit(a[1], a[0], 16),
                                   __builtin_amdgcn_alignbit(a[2], a[1], 16), __builtin_amdgcn_alignbit(a[3], a[2], 16)};
                if (e < 0) w = u32x4{0u, 0u, 0u, 0u};
                *(LAS u32x4*)(hb + n * 16) = w;
            }
        }
        if (lane < 15) *(LAS u32x4*)(lds + L_U + cch * U_CH + cpy * U_G + 2048 + lane * 16) = u32x4{0u, 0u, 0u, 0u};
        WAIT_VM0();
        if (lane == 0) st4_dev(p.ws + WS_UF + ((hg * 8 + wid) * 32 + rt) * 4, TAG);
    }
    STAMP(3);

    f32x16 oacc = {};
    float linv;
    const lptr kvp = lds + L_KV + lane * 16;
    auto side = [&](int s) {
        if (s + 2 < 16) {
            const u32 sl = (u32)((s + 2) % 3) * SLOT;
            dma_dev(vl16, kvg + (size_t)(s + 2) * 16384, kvl + sl); dma_dev(vl16, kvg + (size_t)(s + 2) * 16384 + 1024, kvl + sl + 1024);
        }
        if (s == 12) {
            wait_words<32>(p.ws + WS_UF + (size_t)(bid >> 2) * 128, TAG, lane);
            const int cch = wid & 3, par = wid >> 2;
            const char* ug = p.ws + WS_UT + (size_t)(4 * bid + cch) * 4096 + par * 16;
            const u32 ul = ldsb + L_U + cch * U_CH + par * U_G;
            dma_dev(lane * 32, ug, ul); dma_dev(lane * 32, ug + 2048, ul + 1024);
        }
    };
    if (fast) {
        const short one = ((lane & 1) == ((lane >> 4) & 1)) ? (short)0x3f80 : (short)0;
        bf16x8 ones = {one, one, one, one, one, one, one, one};
        PIN(ones);
        f32x4 lacc = {0.f, 0.f, 0.f, 0.f};
        const f32x16 zero = {};
        WAIT_BAR(0);
        f32x16 sA, sB;
        bf16x8 k0f = ldsb128(kvp), k1f = ldsb128(kvp + 1024);
        sA = MFMA32(k0f, qf0, zero); sA = MFMA32(k1f, qf1, sA);
        k0f = ldsb128(kvp + 4096); k1f = ldsb128(kvp + 4096 + 1024);
        u32x4 pw1 = {0u, 0u, 0u, 0u};
        bf16x8 v1f = {};
        int cur = 0;
#define EX(X, i) X[i] = __builtin_amdgcn_exp2f(X[i])
#define TILE(C, N, VP, KN) do { \
        bf16x8 v0f = ldsb128((VP) + 2048); u32x4 pw0; \
        N = MFMA32(k0f, qf0, zero); EX(C, 0); EX(C, 1); pw0[0] = pk2(C[0], C[1]); EX(C, 2); PIN(C); PIN(pw0); SBAR(); \
        oacc = MFMA32(v1f, __builtin_bit_cast(bf16x8, pw1), oacc); EX(C, 3); pw0[1] = pk2(C[2], C[3]); EX(C, 4); EX(C, 5); PIN(C); PIN(pw0); SBAR(); \
        N = MFMA32(k1f, qf1, N); pw0[2] = pk2(C[4], C[5]); EX(C, 6); EX(C, 7); pw0[3] = pk2(C[6], C[7]); PIN(C); PIN(pw0); SBAR(); \
        k0f = ldsb128(KN); k1f = ldsb128((KN) + 1024); \
        lacc = MFMA16(ones, __builtin_bit_cast(bf16x8, pw1), lacc); EX(C, 8); EX(C, 9); PIN(C); SBAR(); \
        v1f = ldsb128((VP) + 3072); \
        oacc = MFMA32(v0f, __builtin_bit_cast(bf16x8, pw0), oacc); pw1[0] = pk2(C[8], C[9]); EX(C, 10); EX(C, 11); pw1[1] = pk2(C[10], C[11]); EX(C, 12); PIN(C); PIN(pw1); SBAR(); \
        lacc = MFMA16(ones, __builtin_bit_cast(bf16x8, pw0), lacc); EX(C, 13); pw1[2] = pk2(C[12], C[13]); EX(C, 14); EX(C, 15); pw1[3] = pk2(C[14], C[15]); PIN(pw1); SBAR(); \
    } while (0)
        for (int s = 0; s < 16; ++s) {
            side(s);
            const int nxt = cur == 2 * SLOT ? 0 : cur + SLOT;
            const lptr c = kvp + cur, n = kvp + nxt;
            SBAR();
            TILE(sA, sB, c, c + 8192);
            TILE(sB, sA, c + 4096, c + 12288);
            TILE(sA, sB, c + 8192, n);
            TILE(sB, sA, c + 12288, n + 4096);
            cur = nxt;
            WAIT_BAR(0);
        }
        oacc = MFMA32(v1f, __builtin_bit_cast(bf16x8, pw1), oacc);
        lacc = MFMA16(ones, __builtin_bit_cast(bf16x8, pw1), lacc);
        linv = 1.f / ((lane & 16) ? lacc[1] : lacc[0]);
#undef TILE
#undef EX
    } else {
        float m = -__builtin_inff(), l = 0.f;
        const f32x16 zero = {};
        int cur = 0;
        WAIT_BAR(0);
        for (int s = 0; s < 16; ++s) {
            side(s);
            for (int t = 0; t < 4; ++t) {
                const lptr c = kvp + cur + t * 4096;
                f32x16 sc = MFMA32(ldsb128(c), qf0, zero);
                sc = MFMA32(ldsb128(c + 1024), qf1, sc);
                float mx = sc[0];
#pragma unroll
                for (int i = 1; i < 16; ++i) mx = fmaxf(mx, sc[i]);
                const float mn = fmaxf(m, swap32max(mx));
                const float al = __builtin_amdgcn_exp2f(m - mn);
                m = mn;
                float ps = 0.f;
#pragma unroll
                for (int i = 0; i < 16; ++i) { sc[i] = __builtin_amdgcn_exp2f(sc[i] - mn); ps += sc[i]; oacc[i] *= al; }
                l = l * al + ps;
                const u32x4 a = {pk2(sc[0], sc[1]), pk2(sc[2], sc[3]), pk2(sc[4], sc[5]), pk2(sc[6], sc[7])};
                const u32x4 b = {pk2(sc[8], sc[9]), pk2(sc[10], sc[11]), pk2(sc[12], sc[13]), pk2(sc[14], sc[15])};
                oacc = MFMA32(ldsb128(c + 2048), __builtin_bit_cast(bf16x8, a), oacc);
                oacc = MFMA32(ldsb128(c + 3072), __builtin_bit_cast(bf16x8, b), oacc);
            }
            cur = cur == 2 * SLOT ? 0 : cur + SLOT;
            WAIT_BAR(0);
        }
        linv = 1.f / swap32sum(l);
    }
    STAMP(4);
    {
        const char* og = p.ws + WS_O + (size_t)((rt * 32 + hg * 4 + hd) * 2 + rh) * 2048;
        st16_dev(vl16, og, u32x4{pk2(oacc[0] * linv, oacc[1] * linv), pk2(oacc[2] * linv, oacc[3] * linv), pk2(oacc[4] * linv, oacc[5] * linv), pk2(oacc[6] * linv, oacc[7] * linv)});
        st16_dev(vl16, og + 1024, u32x4{pk2(oacc[8] * linv, oacc[9] * linv), pk2(oacc[10] * linv, oacc[11] * linv), pk2(oacc[12] * linv, oacc[13] * linv), pk2(oacc[14] * linv, oacc[15] * linv)});
        WAIT_VM0();
        if (lane == 0) st4_dev(p.ws + WS_OF + (rt * 64 + hg * 8 + wid) * 4, TAG);
    }
    STAMP(5);

    u32 voC[3];
    const char* sbC[3];
    u32 stC[3];
#pragma unroll
    for (int i = 0; i < 3; ++i) {
        const int pc = wid * 3 + i;
        if (pc < 8) { voC[i] = vl16; sbC[i] = p.ws + WS_O + (size_t)rt * 131072 + pc * 1024; stC[i] = 8192; }
        else {
            const int q = pc - 8;
            voC[i] = (4 * q + (lane >> 4)) * 2048 + (((lane & 15) ^ (((lane >> 4) << 2) | (q & 3))) * 16);
            sbC[i] = (const char*)p.wo + hg * 256; stC[i] = 64 * 2048;
        }
    }
    if (wid >= 4) {
        const int hw = wid - 4;
#pragma unroll
        for (int j = 0; j < 3; ++j)
#pragma unroll
            for (int i = 0; i < 4; ++i) {
                const int q = 4 * hw + i;
                dma((4 * q + (lane >> 4)) * 2048 + (((lane & 15) ^ (((lane >> 4) << 2) | i)) * 16), (const char*)p.wo + hg * 256 + (size_t)j * 64 * 2048,
                    ldsb + (2 + j) * SLOT + 8192 + q * 1024);
            }
        wait_words<64>(p.ws + WS_OF + rt * 256, TAG, lane);
#pragma unroll
        for (int j = 0; j < 3; ++j)
#pragma unroll
            for (int i = 0; i < 2; ++i)
                dma_dev(vl16, p.ws + WS_O + (size_t)rt * 131072 + j * 8192 + (2 * hw + i) * 1024, ldsb + (2 + j) * SLOT + (2 * hw + i) * 1024);
    } else {
        const int al = (lane >> 4) & 1, m = lane & 15;
        const lptr ap = lds + L_H + (wid * H_CH + ((m & 1) ? 0 : H_CS) + al * H_SEG + ((m + 2) >> 1) + 4 * h) * 4;
        const lptr bp = lds + L_U + wid * U_CH + h * U_G + 16 * (127 - 16 * al - m);
        f32x16 c0 = {}, c1 = {}, c2 = {}, c3 = {};
#pragma unroll 2
        for (int kp = 0; kp < 16; ++kp) {
            bf16x8 a[4], b[4];
#pragma unroll
            for (int i = 0; i < 4; ++i) {
                const LAS u32* q = (const LAS u32*)(ap + (2 * i * H_SEG + 8 * kp) * 4);
                a[i] = __builtin_bit_cast(bf16x8, (u32x4){q[0], q[1], q[2], q[3]});
                b[i] = ldsb128(bp + 16 * kp - 512 * i);
            }
            c3 = MFMA32(a[0], b[3], c3); c2 = MFMA32(a[0], b[2], c2); c1 = MFMA32(a[0], b[1], c1); c0 = MFMA32(a[0], b[0], c0);
            c3 = MFMA32(a[1], b[2], c3); c2 = MFMA32(a[1], b[1], c2); c1 = MFMA32(a[1], b[0], c1);
            c3 = MFMA32(a[2], b[1], c3); c2 = MFMA32(a[2], b[0], c2);
            c3 = MFMA32(a[3], b[0], c3);
        }
        float y[8][4];
        auto fold = [&](const f32x16& c, int C) {
#pragma unroll
            for (int i = 0; i < 4; ++i) {
                auto x = __builtin_amdgcn_permlane16_swap(__float_as_uint(c[i]), __float_as_uint(c[i + 4]), false, false);
                auto r = __builtin_amdgcn_permlane16_swap(__float_as_uint(c[8 + i]), __float_as_uint(c[12 + i]), false, false);
                if (C == 0) y[0][i] = __uint_as_float(x[0]); else y[2 * C][i] += __uint_as_float(x[0]);
                y[2 * C + 1][i] = __uint_as_float(x[1]) + __uint_as_float(r[0]);
                if (C < 3) y[2 * C + 2][i] = __uint_as_float(r[1]);
            }
        };
        fold(c0, 0); fold(c1, 1); fold(c2, 2); fold(c3, 3);
        const int ch = 4 * bid + wid;
        const float dd = __uint_as_float((u32)p.ssmd[ch] << 16);
        const int tl = 16 * m + 8 * al + 4 * h;
        const char* zg = p.ws + WS_ZT + (size_t)ch * 4096;
#pragma unroll
        for (int c = 0; c < 8; ++c) {
            const int xr = 2044 - 256 * c - tl;
            const u32x2 uu = *(const LAS u32x2*)(lds + L_U + wid * U_CH + ((xr >> 3) & 1) * U_G + (xr >> 4) * 16 + (xr & 4) * 2);
            const float z0 = fmaf(bfhi(uu[1]), dd, y[c][0]), z1 = fmaf(bflo(uu[1]), dd, y[c][1]);
            const float z2 = fmaf(bfhi(uu[0]), dd, y[c][2]), z3 = fmaf(bflo(uu[0]), dd, y[c][3]);
            st8_dev((u32)(256 * c + tl) * 2, zg, u32x2{pk2(z0, z1), pk2(z2, z3)});
        }
        WAIT_VM0();
        if (lane == 0) st4_dev(p.ws + WS_ZF + ch * 4, TAG);
    }
    STAMP(6);

    f32x16 bacc = {};
    {
        WAIT_BAR(0);
        auto issue3 = [&](auto J) {
            constexpr int j = J.value;
#pragma unroll
            for (int i = 0; i < 3; ++i) {
                const u32 dst = ldsb + ((2 + j) % 6) * SLOT + (wid * 3 + i) * 1024;
                if (wid * 3 + i < 8) dma_dev(voC[i], sbC[i] + (size_t)j * stC[i], dst); else dma(voC[i], sbC[i] + (size_t)j * stC[i], dst);
            }
        };
        static_for<3, 6>(issue3);
        const int blk = (lane >> 4) & 1, q = (lane & 15) >> 2, pp = lane & 3;
        const int ch = 4 * hd + 2 * blk + (pp >> 1);
        wp0 = lds + 8192 + 256 * (4 * h + q) + 16 * (ch ^ ((q << 2) | h)) + 8 * (pp & 1);
        wp1 = lds + 8192 + 256 * (8 + 4 * h + q) + 16 * (ch ^ ((q << 2) | (2 + h))) + 8 * (pp & 1);
#pragma unroll
        for (int ks = 0; ks < 4; ++ks) xp[ks] = lds + (ks >> 1) * 4096 + rh * 2048 + (ks & 1) * 1024 + lane * 16;
        static_for<0, 16>([&](auto J) {
            constexpr int j = J.value;
            constexpr int last = j == 0 ? 5 : (j + 4 < 15 ? j + 4 : 15);
            constexpr int nz = j == 12 + 1 ? 0 : 0;
            if constexpr (j > 0) WAIT_BAR((j < 3 ? 3 * (last - 2) : 3 * (last - j)) + nz + (j > 12 ? 2 : 0));
            if constexpr (j >= 1 && j + 5 <= 15) issue3(std::integral_constant<int, j + 5>{});
            if constexpr (j == 12) {
                wait_words<128>(p.ws + WS_ZF + hg * 512, TAG, lane);
#pragma unroll
                for (int i = 0; i < 2; ++i) {
                    const int row = 8 * (2 * wid + i) + (lane >> 3);
                    dma_dev(row * 4096 + (((lane & 7) ^ (((row >> 1) & 1) << 2)) * 16), p.ws + WS_ZT + (size_t)hg * 128 * 4096 + rt * 128,
                            ldsb + ((2 + 11) % 6) * SLOT + (2 * wid + i) * 1024);
                }
            }
            gemm_stage<false>(bacc, wp0, wp1, xp, ((2 + j) % 6) * SLOT);
        });
    }
    STAMP(7);
    {
        const int t = 64 * rt + 32 * rh + r32, cb = 128 * hg + 32 * hd;
        u32x2 xv[4];
#pragma unroll
        for (int i = 0; i < 4; ++i) xv[i] = *(const u32x2*)(p.x + (size_t)t * D + cb + 8 * i + 4 * h);
        const float g = __uint_as_float(ld4_dev(p.ws + WS_G + t * 4));
        WAIT_BAR(0);
        const int q = (lane & 15) >> 2, pp = lane & 3, blk = (lane >> 4) & 1;
        u32x2 ov[4];
#pragma unroll
        for (int i = 0; i < 4; ++i) {
            const int row = 32 * hd + 8 * i + 4 * h + q;
            const int chunk = 4 * rh + 2 * blk + (pp >> 1);
            const s16x4 z = ldstr(lds + ((2 + 11) % 6) * SLOT + row * 128 + 16 * (chunk ^ (((row >> 1) & 1) << 2)) + 8 * (pp & 1));
            const u32x2 zz = __builtin_bit_cast(u32x2, z);
            const float g1 = 1.f - g;
            const float o0 = fmaf(g * bflo(xv[i][0]), bflo(zz[0]), g1 * bacc[4 * i]);
            const float o1 = fmaf(g * bfhi(xv[i][0]), bfhi(zz[0]), g1 * bacc[4 * i + 1]);
            const float o2 = fmaf(g * bflo(xv[i][1]), bflo(zz[1]), g1 * bacc[4 * i + 2]);
            const float o3 = fmaf(g * bfhi(xv[i][1]), bfhi(zz[1]), g1 * bacc[4 * i + 3]);
            ov[i] = u32x2{pk2(o0, o1), pk2(o2, o3)};
        }
#pragma unroll
        for (int i = 0; i < 4; i += 2) {
            auto a = __builtin_amdgcn_permlane32_swap(ov[i][0], ov[i + 1][0], false, false);
            auto b = __builtin_amdgcn_permlane32_swap(ov[i][1], ov[i + 1][1], false, false);
            *(u32x4*)(p.out + (size_t)t * D + cb + 8 * i + 8 * h) = u32x4{a[0], b[0], a[1], b[1]};
        }
    }
    STAMP(8);
}

extern "C" void kernel_launch(void* const* d_in, const int* in_sizes, int n_in, void* d_out, int out_size, void* d_ws, size_t ws_size, hipStream_t stream) {
    Params p;
    p.x = (const u16*)d_in[0]; p.k = (const u16*)d_in[1]; p.v = (const u16*)d_in[2]; p.sk = (const u16*)d_in[3];
    p.ssmk = (const u16*)d_in[4]; p.ssmd = (const u16*)d_in[5]; p.wq = (const u16*)d_in[7]; p.wk = (const u16*)d_in[8];
    p.wv = (const u16*)d_in[9]; p.wo = (const u16*)d_in[10]; p.gw = (const u16*)d_in[11];
    p.out = (u16*)d_out; p.ws = (char*)d_ws;
    (void)hipFuncSetAttribute((const void*)fused_kernel, hipFuncAttributeMaxDynamicSharedMemorySize, LDS_BYTES);
    (void)hipMemsetAsync(d_ws, 0, WS_FLAG_BYTES, stream);
    fused_kernel<<<256, NT, LDS_BYTES, stream>>>(p);
}
```

```cpp
#define PADAT 99
#include <hip/hip_runtime.h>
#include <stdint.h>

typedef unsigned short u16;
typedef unsigned int u32;
typedef unsigned long long u64;
using bf16x8 = __attribute__((ext_vector_type(8))) short;
using s16x4 = __attribute__((ext_vector_type(4))) short;
using f32x16 = __attribute__((ext_vector_type(16))) float;
using f32x4 = __attribute__((ext_vector_type(4))) float;
using f32x2 = __attribute__((ext_vector_type(2))) float;
using bf16x2 = __attribute__((ext_vector_type(2))) __bf16;
using u32x4 = __attribute__((ext_vector_type(4))) unsigned;
using u32x2 = __attribute__((ext_vector_type(2))) unsigned;
#define LAS __attribute__((address_space(3)))
#define GAS __attribute__((address_space(1)))
typedef LAS char* lptr;

constexpr int S = 2048, D = 1024;
constexpr int NT = 512;

constexpr size_t WS_KVF = 0;
constexpr size_t WS_UF = 4096;
constexpr size_t WS_OF = 12288;
constexpr size_t WS_ZF = 20480;
constexpr size_t WS_FLAG_BYTES = 24576;
constexpr size_t WS_G = 32768;
constexpr size_t WS_KV = 65536;
constexpr size_t WS_UT = 1u << 20;
constexpr size_t WS_O = 5u << 20;
constexpr size_t WS_ZT = 9u << 20;
constexpr size_t WS_TS = 13u << 20;

constexpr int SLOT = 24576;
constexpr int L_MISC = 6 * SLOT;
constexpr int LDS_BYTES = 163840;
constexpr int L_H = 0;
constexpr int H_SEG = 136, H_CS = 1104, H_CH = 2192;
constexpr int L_U = 5 * SLOT;
constexpr int U_G = 2288, U_CH = 4576;
constexpr int L_KV = 2 * SLOT;

constexpr float QSCALE = 0.17677669529663687f * 1.4426950408889634f;
constexpr float SAFE_T2 = 64.f * 64.f;
constexpr float SAFE_V = 1099511627776.f;
constexpr u32 SPIN_CAP = 1u << 16;

struct Params {
    const u16 *x, *k, *v, *sk, *ssmk, *ssmd, *wq, *wk, *wv, *wo, *gw;
    u16* out;
    char* ws;
};

#define SBAR() __builtin_amdgcn_sched_barrier(0)
#define PIN(x) asm volatile("" : "+v"(x))
#define MFMA32(a, b, c) __builtin_amdgcn_mfma_f32_32x32x16_bf16(a, b, c, 0, 0, 0)
#define MFMA16(a, b, c) __builtin_amdgcn_mfma_f32_16x16x32_bf16(a, b, c, 0, 0, 0)
#define WAIT_BAR(N) asm volatile("s_waitcnt vmcnt(%c0) lgkmcnt(0)\n\ts_barrier" ::"i"(N) : "memory")
#define WAIT_VM0() asm volatile("s_waitcnt vmcnt(0)" ::: "memory")

#define STAMP(i) do { if ((i) == PADAT) { while (__builtin_amdgcn_s_memrealtime() - tstart_ < 7000ull) __builtin_amdgcn_s_sleep(1); } } while (0)

__device__ __forceinline__ u32 rfl(u32 v) { return __builtin_amdgcn_readfirstlane(v); }
__device__ __forceinline__ float bflo(u32 v) { return __uint_as_float(v << 16); }
__device__ __forceinline__ float bfhi(u32 v) { return __uint_as_float(v & 0xffff0000u); }
__device__ __forceinline__ u32 pk2(float lo, float hi) {
    f32x2 f = {lo, hi};
    return __builtin_bit_cast(u32, __builtin_convertvector(f, bf16x2));
}
__device__ __forceinline__ void dma(u32 voff, const void* sbase, u32 ldsaddr) {
    asm volatile("s_mov_b32 m0, %2\n\ts_nop 0\n\tglobal_load_lds_dwordx4 %0, %1" ::"v"(voff), "s"(sbase), "s"(ldsaddr) : "memory");
}
__device__ __forceinline__ void dma_dev(u32 voff, const void* sbase, u32 ldsaddr) {
    asm volatile("s_mov_b32 m0, %2\n\ts_nop 0\n\tglobal_load_lds_dwordx4 %0, %1 sc1" ::"v"(voff), "s"(sbase), "s"(ldsaddr) : "memory");
}
__device__ __forceinline__ void st16_dev(u32 voff, const void* sbase, u32x4 v) {
    asm volatile("global_store_dwordx4 %0, %1, %2 sc1\n\ts_nop 1" ::"v"(voff), "v"(v), "s"(sbase) : "memory");
}
__device__ __forceinline__ void st8_dev(u32 voff, const void* sbase, u32x2 v) {
    asm volatile("global_store_dwordx2 %0, %1, %2 sc1" ::"v"(voff), "v"(v), "s"(sbase) : "memory");
}
__device__ __forceinline__ void st4_dev(void* ptr, u32 v) { __hip_atomic_store((GAS u32*)ptr, v, __ATOMIC_RELAXED, __HIP_MEMORY_SCOPE_AGENT); }
__device__ __forceinline__ void st8g_dev(void* ptr, u64 v) { __hip_atomic_store((GAS u64*)ptr, v, __ATOMIC_RELAXED, __HIP_MEMORY_SCOPE_AGENT); }
__device__ __forceinline__ u32 ld4_dev(const void* ptr) { return __hip_atomic_load((GAS const u32*)ptr, __ATOMIC_RELAXED, __HIP_MEMORY_SCOPE_AGENT); }
__device__ __forceinline__ u64 ld8_dev(const void* ptr) { return __hip_atomic_load((GAS const u64*)ptr, __ATOMIC_RELAXED, __HIP_MEMORY_SCOPE_AGENT); }

__device__ __forceinline__ bf16x8 ldsb128(lptr p) { return *(const LAS bf16x8*)p; }
typedef short v4i16_t __attribute__((ext_vector_type(4)));
__device__ __forceinline__ s16x4 ldstr(lptr p) { return __builtin_bit_cast(s16x4, __builtin_amdgcn_ds_read_tr16_b64_v4i16((LAS v4i16_t*)p)); }
__device__ __forceinline__ bf16x8 cat8(s16x4 a, s16x4 b) { return (bf16x8){a[0], a[1], a[2], a[3], b[0], b[1], b[2], b[3]}; }
__device__ __forceinline__ float swap32sum(float v) {
    auto r = __builtin_amdgcn_permlane32_swap(__float_as_uint(v), __float_as_uint(v), false, false);
    return __uint_as_float(r[0]) + __uint_as_float(r[1]);
}
__device__ __forceinline__ float swap32max(float v) {
    auto r = __builtin_amdgcn_permlane32_swap(__float_as_uint(v), __float_as_uint(v), false, false);
    return fmaxf(__uint_as_float(r[0]), __uint_as_float(r[1]));
}

template <int N>
__device__ __forceinline__ void wait_words(const char* base, u32 tag, int lane) {
    for (u32 spins = 0; spins < SPIN_CAP; ++spins) {
        bool ok = true;
        if (N <= 64) ok = ld4_dev(base + (lane & (N - 1)) * 4) - tag + 300u < 600u;
        else { const u64 v = ld8_dev(base + lane * 8); ok = (u32)v - tag + 300u < 600u && (u32)(v >> 32) - tag + 300u < 600u; }
        if (__all(ok)) break;
        __builtin_amdgcn_s_sleep(1);
    }
    __builtin_amdgcn_fence(__ATOMIC_ACQUIRE, "wavefront");
}

template <bool NATURAL_K>
__device__ __forceinline__ void gemm_stage(f32x16& acc, lptr wp0, lptr wp1, const lptr (&xp)[4], int off) {
#pragma unroll
    for (int ks = 0; ks < 4; ++ks) {
        const bf16x8 a = cat8(ldstr(wp0 + off + ks * 4096), ldstr(wp1 + off + ks * 4096));
        const bf16x8 b = ldsb128(xp[ks] + off);
        acc = MFMA32(a, b, acc);
    }
}

template <int I, int N, class F>
__device__ __forceinline__ void static_for(F&& f) {
    if constexpr (I < N) { f(std::integral_constant<int, I>{}); static_for<I + 1, N>(f); }
}

__global__ __launch_bounds__(NT, 2) void fused_kernel(Params p) {
    extern __shared__ __attribute__((aligned(16))) char smem[];
    const lptr lds = (lptr)smem;
    const u32 ldsb = (u32)(size_t)smem;
    const int tid = threadIdx.x, lane = tid & 63;
    const int wid = rfl(tid >> 6);
    const int bid = blockIdx.x;
    const int xcd = bid & 7, ix = bid >> 3;
    const int rt = xcd * 4 + (ix >> 3), hg = ix & 7;
    const u32 TAG = (u32)__builtin_amdgcn_s_memrealtime();
    const int r32 = lane & 31, h = lane >> 5;
    const u64 tstart_ = __builtin_amdgcn_s_memrealtime();
    STAMP(0);

    const int cg = hg >> 1;
    const bool dmaw = wid >= 1 && wid <= 6;
    const int dw = wid - 1;
    u32 voA[4], voB[4];
    const char *sbA, *sbB;
    u32 stA, stB;
    if (dw < 4) {
#pragma unroll
        for (int i = 0; i < 4; ++i) { const int row = (8 * dw + 2 * i + h) & 31; voA[i] = row * 2048 + ((r32 ^ (row & 15)) * 16); }
        sbA = (const char*)p.x + (size_t)(64 * rt + 32 * (hg & 1)) * 2048; stA = 512;
    } else {
#pragma unroll
        for (int i = 0; i < 4; ++i) voA[i] = ((((dw - 4) * 4 + i) & 7) * 32 + (lane >> 1)) * 64 + (lane & 1) * 16;
        sbA = (const char*)(cg < 2 ? p.wk : p.wv) + (cg & 1) * 32; stA = 256 * 64;
    }
    if (dw < 2) {
#pragma unroll
        for (int i = 0; i < 4; ++i) { const int row = (8 * (4 * dw + i) + (lane >> 3)) & 63; voB[i] = row * 2048 + (((lane & 7) ^ ((row >> 1) & 7)) * 16); }
        sbB = (const char*)p.x + (size_t)(64 * rt) * 2048; stB = 128;
    } else {
#pragma unroll
        for (int i = 0; i < 4; ++i) { const int row = 4 * ((4 * (dw - 2) + i) & 15) + (lane >> 4); voB[i] = row * 2048 + (((lane & 15) ^ (((lane >> 4) << 2) | i)) * 16); }
        sbB = (const char*)p.wq + hg * 256; stB = 64 * 2048;
    }
    const u32 ldsw = ldsb + (dw & 7) * 4096;
    auto issue0 = [&](auto SG) {
        constexpr int sg = SG.value;
        if (dmaw) {
#pragma unroll
            for (int i = 0; i < 4; ++i) {
                if constexpr (sg < 4) dma(voA[i], sbA + (size_t)sg * stA, ldsw + (sg % 6) * SLOT + i * 1024);
                else dma(voB[i], sbB + (size_t)(sg - 4) * stB, ldsw + (sg % 6) * SLOT + i * 1024);
            }
        }
    };
    static_for<0, 6>(issue0);

    const int hd = wid >> 1, rh = wid & 1;
    lptr wp0, wp1, xp[4];
    {
        const int blk = (lane >> 4) & 1, q = (lane & 15) >> 2, pp = lane & 3;
        const int ch = 4 * hd + 2 * blk + (pp >> 1);
        wp0 = lds + 8192 + 256 * (8 * h + q) + 16 * (ch ^ ((q << 2) | (2 * h))) + 8 * (pp & 1);
        wp1 = lds + 8192 + 256 * (8 * h + 4 + q) + 16 * (ch ^ ((q << 2) | (2 * h + 1))) + 8 * (pp & 1);
        const int row = 32 * rh + r32;
#pragma unroll
        for (int ks = 0; ks < 4; ++ks) xp[ks] = lds + row * 128 + 16 * ((2 * ks + h) ^ ((row >> 1) & 7));
    }
    const bool kvw = wid == 0 || wid == 7;
    const int ku = wid == 7 ? 1 : 0;
    f32x4 kvacc = {0.f, 0.f, 0.f, 0.f};
    f32x16 qacc = {};
    float kvstat = 0.f;

    static_for<0, 20>([&](auto SG) {
        constexpr int sg = SG.value;
        constexpr int nout = sg == 0 ? 5 : (sg <= 15 ? 4 : 19 - sg);
        WAIT_BAR(4 * nout);
        if constexpr (sg >= 1 && sg + 5 <= 19) issue0(std::integral_constant<int, sg + 5>{});
        constexpr int off = (sg % 6) * SLOT;
        if constexpr (sg < 4) {
            if (kvw) {
                const int g = lane >> 4, row = 16 * ku + (lane & 15);
                const lptr xr = lds + off + row * 512;
                const lptr wr = lds + off + 16384 + (8 * g + ((lane & 15) >> 2)) * 32 + 8 * (lane & 3);
#pragma unroll
                for (int ks = 0; ks < 8; ++ks) {
                    const bf16x8 xf = ldsb128(xr + 16 * ((4 * ks + g) ^ (row & 15)));
                    const bf16x8 wf = cat8(ldstr(wr + ks * 1024), ldstr(wr + ks * 1024 + 128));
                    if (cg < 2) kvacc = MFMA16(wf, xf, kvacc); else kvacc = MFMA16(xf, wf, kvacc);
                }
                if constexpr (sg == 3) {
                    const int tile = 2 * rt + (hg & 1), c15 = lane & 15;
                    const u32 o = cg < 2 ? (u32)(cg * 1024 + ((g & 1) * 32 + 16 * ku + c15) * 16 + (g >> 1) * 8)
                                         : (u32)(2048 + ku * 1024 + ((g & 1) * 32 + 16 * (cg - 2) + c15) * 16 + (g >> 1) * 8);
                    st8_dev(o, p.ws + WS_KV + (size_t)tile * 4096, u32x2{pk2(kvacc[0], kvacc[1]), pk2(kvacc[2], kvacc[3])});
                    float m;
                    if (cg < 2) {
                        m = kvacc[0] * kvacc[0] + kvacc[1] * kvacc[1] + kvacc[2] * kvacc[2] + kvacc[3] * kvacc[3];
                        m += __shfl_xor(m, 16); m += __shfl_xor(m, 32);
                    } else {
                        m = __uint_as_float(max(max(__float_as_uint(kvacc[0]) & 0x7fffffffu, __float_as_uint(kvacc[1]) & 0x7fffffffu),
                                                max(__float_as_uint(kvacc[2]) & 0x7fffffffu, __float_as_uint(kvacc[3]) & 0x7fffffffu)));
                    }
                    u32 mb = __float_as_uint(m) & 0x7fffffffu;
#pragma unroll
                    for (int o2 = 1; o2 < 64; o2 <<= 1) mb = max(mb, (u32)__shfl_xor((int)mb, o2));
                    kvstat = __uint_as_float(mb);
                }
            }
        } else {
            gemm_stage<true>(qacc, wp0, wp1, xp, off);
            if constexpr (sg == 5) {
                if (kvw) {
                    WAIT_VM0();
                    if (lane == 0) st8g_dev(p.ws + WS_KVF + (size_t)((rt * 8 + hg) * 2 + ku) * 8, ((u64)__float_as_uint(kvstat) << 32) | TAG);
                }
            }
        }
    });
    STAMP(1);
    WAIT_BAR(0);

    bf16x8 qf0, qf1;
    float q2 = 0.f;
    {
        float qs[16];
#pragma unroll
        for (int i = 0; i < 16; ++i) { qs[i] = qacc[i] * QSCALE; q2 = fmaf(qs[i], qs[i], q2); }
        q2 = swap32sum(q2);
        qf0 = __builtin_bit_cast(bf16x8, (u32x4){pk2(qs[0], qs[1]), pk2(qs[2], qs[3]), pk2(qs[4], qs[5]), pk2(qs[6], qs[7])});
        qf1 = __builtin_bit_cast(bf16x8, (u32x4){pk2(qs[8], qs[9]), pk2(qs[10], qs[11]), pk2(qs[12], qs[13]), pk2(qs[14], qs[15])});
    }

    bool fast;
    {
        u32 k0 = 0, k1 = 0, vm = 0;
        for (u32 spins = 0; spins < SPIN_CAP; ++spins) {
            bool ok = true; k0 = 0; k1 = 0; vm = 0;
#pragma unroll
            for (int i = 0; i < 8; ++i) {
                const u64 gv = ld8_dev(p.ws + WS_KVF + (size_t)(i * 64 + lane) * 8);
                ok &= (u32)gv - TAG + 300u < 600u;
                const u32 val = (u32)(gv >> 32);
                const int c = (lane >> 2) & 3;
                k0 = max(k0, c == 0 ? val : 0u); k1 = max(k1, c == 1 ? val : 0u); vm = max(vm, c >= 2 ? val : 0u);
            }
            if (__all(ok)) break;
            __builtin_amdgcn_s_sleep(1);
        }
        __builtin_amdgcn_fence(__ATOMIC_ACQUIRE, "wavefront");
#pragma unroll
        for (int o2 = 1; o2 < 64; o2 <<= 1) {
            k0 = max(k0, (u32)__shfl_xor((int)k0, o2)); k1 = max(k1, (u32)__shfl_xor((int)k1, o2)); vm = max(vm, (u32)__shfl_xor((int)vm, o2));
        }
        const float k2 = __uint_as_float(k0) + __uint_as_float(k1);
        fast = __all(q2 * k2 <= SAFE_T2 && __uint_as_float(vm) <= SAFE_V);
    }
    const char* kvg = p.ws + WS_KV + wid * 2048;
    const u32 kvl = ldsb + L_KV + wid * 2048;
    const u32 vl16 = lane * 16;
    dma_dev(vl16, kvg, kvl); dma_dev(vl16, kvg + 1024, kvl + 1024);
    dma_dev(vl16, kvg + 16384, kvl + SLOT); dma_dev(vl16, kvg + 16384 + 1024, kvl + SLOT + 1024);
    STAMP(2);

    {
        const int cp = lane & 7, tg = lane >> 3;
        const int c0 = 128 * hg + 16 * wid + 2 * cp, t0 = 64 * rt + 8 * tg;
        u32 kr[11], vr[8];
#pragma unroll
        for (int i = 0; i < 11; ++i) { const int t = t0 - 3 + i; kr[i] = *(const u32*)(p.k + (size_t)max(t, 0) * D + c0); if (i < 3 && t < 0) kr[i] = 0u; }
#pragma unroll
        for (int i = 0; i < 8; ++i) vr[i] = *(const u32*)(p.v + (size_t)(t0 + i) * D + c0);
        const u32x4 sw = *(const u32x4*)(p.sk + c0 * 4);
        const int grow = 64 * rt + 8 * hg + wid;
        const u32x4 gx0 = *(const u32x4*)(p.x + (size_t)grow * D + lane * 8), gx1 = *(const u32x4*)(p.x + (size_t)grow * D + 512 + lane * 8);
        const u32x4 gw0 = *(const u32x4*)(p.gw + lane * 8), gw1 = *(const u32x4*)(p.gw + 512 + lane * 8);
        float u0[8], u1[8];
#pragma unroll
        for (int i = 0; i < 8; ++i) {
            const float a = fmaf(bflo(sw[0]), bflo(kr[i + 3]), fmaf(bfhi(sw[0]), bflo(kr[i + 2]), fmaf(bflo(sw[1]), bflo(kr[i + 1]), bfhi(sw[1]) * bflo(kr[i]))));
            const float b = fmaf(bflo(sw[2]), bfhi(kr[i + 3]), fmaf(bfhi(sw[2]), bfhi(kr[i + 2]), fmaf(bflo(sw[3]), bfhi(kr[i + 1]), bfhi(sw[3]) * bfhi(kr[i]))));
            u0[i] = a * bflo(vr[i]); u1[i] = b * bfhi(vr[i]);
        }
        const u32 uo = (u32)(c0 * 4096 + (2040 - t0) * 2);
        st16_dev(uo, p.ws + WS_UT, u32x4{pk2(u0[7], u0[6]), pk2(u0[5], u0[4]), pk2(u0[3], u0[2]), pk2(u0[1], u0[0])});
        st16_dev(uo + 4096, p.ws + WS_UT, u32x4{pk2(u1[7], u1[6]), pk2(u1[5], u1[4]), pk2(u1[3], u1[2]), pk2(u1[1], u1[0])});
        float gs = 0.f;
#pragma unroll
        for (int i = 0; i < 4; ++i) {
            gs = fmaf(bflo(gx0[i]), bflo(gw0[i]), gs); gs = fmaf(bfhi(gx0[i]), bfhi(gw0[i]), gs);
            gs = fmaf(bflo(gx1[i]), bflo(gw1[i]), gs); gs = fmaf(bfhi(gx1[i]), bfhi(gw1[i]), gs);
        }
#pragma unroll
        for (int o2 = 1; o2 < 64; o2 <<= 1) gs += __shfl_xor(gs, o2);
        if (lane == 0) st4_dev(p.ws + WS_G + grow * 4, __float_as_uint(1.f / (1.f + __expf(-gs))));

        const int cch = wid & 3, cpy = wid >> 2;
        const u16* hrow = p.ssmk + (size_t)(4 * bid + cch) * S;
        const lptr hb = lds + L_H + (cch * H_CH + cpy * H_CS) * 4;
#pragma unroll
        for (int i = 0; i < 5; ++i) {
            const int n = i * 64 + lane;
            if (n < 272) {
                const int seg = n / 34, cc = n - seg * 34;
                const int e = 256 * seg - 16 + 8 * cc;
                const u32x4 a = *(const u32x4*)(hrow + max(e, 0));
                const u32 prev = *(const u32*)(hrow + max(e - 2, 0));
                u32x4 w = a;
                if (cpy) w = u32x4{__builtin_amdgcn_alignbit(a[0], e >= 2 ? prev : 0u, 16), __builtin_amdgcn_alignbit(a[1], a[0], 16),
                                   __builtin_amdgcn_alignbit(a[2], a[1], 16), __builtin_amdgcn_alignbit(a[3], a[2], 16)};
                if (e < 0) w = u32x4{0u, 0u, 0u, 0u};
                *(LAS u32x4*)(hb + n * 16) = w;
            }
        }
        if (lane < 15) *(LAS u32x4*)(lds + L_U + cch * U_CH + cpy * U_G + 2048 + lane * 16) = u32x4{0u, 0u, 0u, 0u};
        WAIT_VM0();
        if (lane == 0) st4_dev(p.ws + WS_UF + ((hg * 8 + wid) * 32 + rt) * 4, TAG);
    }
    STAMP(3);

    f32x16 oacc = {};
    float linv;
    const lptr kvp = lds + L_KV + lane * 16;
    auto side = [&](int s) {
        if (s + 2 < 16) {
            const u32 sl = (u32)((s + 2) % 3) * SLOT;
            dma_dev(vl16, kvg + (size_t)(s + 2) * 16384, kvl + sl); dma_dev(vl16, kvg + (size_t)(s + 2) * 16384 + 1024, kvl + sl + 1024);
        }
        if (s == 12) {
            wait_words<32>(p.ws + WS_UF + (size_t)(bid >> 2) * 128, TAG, lane);
            const int cch = wid & 3, par = wid >> 2;
            const char* ug = p.ws + WS_UT + (size_t)(4 * bid + cch) * 4096 + par * 16;
            const u32 ul = ldsb + L_U + cch * U_CH + par * U_G;
            dma_dev(lane * 32, ug, ul); dma_dev(lane * 32, ug + 2048, ul + 1024);
        }
    };
    if (fast) {
        const short one = ((lane & 1) == ((lane >> 4) & 1)) ? (short)0x3f80 : (short)0;
        bf16x8 ones = {one, one, one, one, one, one, one, one};
        PIN(ones);
        f32x4 lacc = {0.f, 0.f, 0.f, 0.f};
        const f32x16 zero = {};
        WAIT_BAR(0);
        f32x16 sA, sB;
        bf16x8 k0f = ldsb128(kvp), k1f = ldsb128(kvp + 1024);
        sA = MFMA32(k0f, qf0, zero); sA = MFMA32(k1f, qf1, sA);
        k0f = ldsb128(kvp + 4096); k1f = ldsb128(kvp + 4096 + 1024);
        u32x4 pw1 = {0u, 0u, 0u, 0u};
        bf16x8 v1f = {};
        int cur = 0;
#define EX(X, i) X[i] = __builtin_amdgcn_exp2f(X[i])
#define TILE(C, N, VP, KN) do { \
        bf16x8 v0f = ldsb128((VP) + 2048); u32x4 pw0; \
        N = MFMA32(k0f, qf0, zero); EX(C, 0); EX(C, 1); pw0[0] = pk2(C[0], C[1]); EX(C, 2); PIN(C); PIN(pw0); SBAR(); \
        oacc = MFMA32(v1f, __builtin_bit_cast(bf16x8, pw1), oacc); EX(C, 3); pw0[1] = pk2(C[2], C[3]); EX(C, 4); EX(C, 5); PIN(C); PIN(pw0); SBAR(); \
        N = MFMA32(k1f, qf1, N); pw0[2] = pk2(C[4], C[5]); EX(C, 6); EX(C, 7); pw0[3] = pk2(C[6], C[7]); PIN(C); PIN(pw0); SBAR(); \
        k0f = ldsb128(KN); k1f = ldsb128((KN) + 1024); \
        lacc = MFMA16(ones, __builtin_bit_cast(bf16x8, pw1), lacc); EX(C, 8); EX(C, 9); PIN(C); SBAR(); \
        v1f = ldsb128((VP) + 3072); \
        oacc = MFMA32(v0f, __builtin_bit_cast(bf16x8, pw0), oacc); pw1[0] = pk2(C[8], C[9]); EX(C, 10); EX(C, 11); pw1[1] = pk2(C[10], C[11]); EX(C, 12); PIN(C); PIN(pw1); SBAR(); \
        lacc = MFMA16(ones, __builtin_bit_cast(bf16x8, pw0), lacc); EX(C, 13); pw1[2] = pk2(C[12], C[13]); EX(C, 14); EX(C, 15); pw1[3] = pk2(C[14], C[15]); PIN(pw1); SBAR(); \
    } while (0)
        for (int s = 0; s < 16; ++s) {
            side(s);
            const int nxt = cur == 2 * SLOT ? 0 : cur + SLOT;
            const lptr c = kvp + cur, n = kvp + nxt;
            SBAR();
            TILE(sA, sB, c, c + 8192);
            TILE(sB, sA, c + 4096, c + 12288);
            TILE(sA, sB, c + 8192, n);
            TILE(sB, sA, c + 12288, n + 4096);
            cur = nxt;
            WAIT_BAR(0);
        }
        oacc = MFMA32(v1f, __builtin_bit_cast(bf16x8, pw1), oacc);
        lacc = MFMA16(ones, __builtin_bit_cast(bf16x8, pw1), lacc);
        linv = 1.f / ((lane & 16) ? lacc[1] : lacc[0]);
#undef TILE
#undef EX
    } else {
        float m = -__builtin_inff(), l = 0.f;
        const f32x16 zero = {};
        int cur = 0;
        WAIT_BAR(0);
        for (int s = 0; s < 16; ++s) {
            side(s);
            for (int t = 0; t < 4; ++t) {
                const lptr c = kvp + cur + t * 4096;
                f32x16 sc = MFMA32(ldsb128(c), qf0, zero);
                sc = MFMA32(ldsb128(c + 1024), qf1, sc);
                float mx = sc[0];
#pragma unroll
                for (int i = 1; i < 16; ++i) mx = fmaxf(mx, sc[i]);
                const float mn = fmaxf(m, swap32max(mx));
                const float al = __builtin_amdgcn_exp2f(m - mn);
                m = mn;
                float ps = 0.f;
#pragma unroll
                for (int i = 0; i < 16; ++i) { sc[i] = __builtin_amdgcn_exp2f(sc[i] - mn); ps += sc[i]; oacc[i] *= al; }
                l = l * al + ps;
                const u32x4 a = {pk2(sc[0], sc[1]), pk2(sc[2], sc[3]), pk2(sc[4], sc[5]), pk2(sc[6], sc[7])};
                const u32x4 b = {pk2(sc[8], sc[9]), pk2(sc[10], sc[11]), pk2(sc[12], sc[13]), pk2(sc[14], sc[15])};
                oacc = MFMA32(ldsb128(c + 2048), __builtin_bit_cast(bf16x8, a), oacc);
                oacc = MFMA32(ldsb128(c + 3072), __builtin_bit_cast(bf16x8, b), oacc);
            }
            cur = cur == 2 * SLOT ? 0 : cur + SLOT;
            WAIT_BAR(0);
        }
        linv = 1.f / swap32sum(l);
    }
    STAMP(4);
    {
        const char* og = p.ws + WS_O + (size_t)((rt * 32 + hg * 4 + hd) * 2 + rh) * 2048;
        st16_dev(vl16, og, u32x4{pk2(oacc[0] * linv, oacc[1] * linv), pk2(oacc[2] * linv, oacc[3] * linv), pk2(oacc[4] * linv, oacc[5] * linv), pk2(oacc[6] * linv, oacc[7] * linv)});
        st16_dev(vl16, og + 1024, u32x4{pk2(oacc[8] * linv, oacc[9] * linv), pk2(oacc[10] * linv, oacc[11] * linv), pk2(oacc[12] * linv, oacc[13] * linv), pk2(oacc[14] * linv, oacc[15] * linv)});
        WAIT_VM0();
        if (lane == 0) st4_dev(p.ws + WS_OF + (rt * 64 + hg * 8 + wid) * 4, TAG);
    }
    STAMP(5);

    u32 voC[3];
    const char* sbC[3];
    u32 stC[3];
#pragma unroll
    for (int i = 0; i < 3; ++i) {
        const int pc = wid * 3 + i;
        if (pc < 8) { voC[i] = vl16; sbC[i] = p.ws + WS_O + (size_t)rt * 131072 + pc * 1024; stC[i] = 8192; }
        else {
            const int q = pc - 8;
            voC[i] = (4 * q + (lane >> 4)) * 2048 + (((lane & 15) ^ (((lane >> 4) << 2) | (q & 3))) * 16);
            sbC[i] = (const char*)p.wo + hg * 256; stC[i] = 64 * 2048;
        }
    }
    if (wid >= 4) {
        const int hw = wid - 4;
#pragma unroll
        for (int j = 0; j < 3; ++j)
#pragma unroll
            for (int i = 0; i < 4; ++i) {
                const int q = 4 * hw + i;
                dma((4 * q + (lane >> 4)) * 2048 + (((lane & 15) ^ (((lane >> 4) << 2) | i)) * 16), (const char*)p.wo + hg * 256 + (size_t)j * 64 * 2048,
                    ldsb + (2 + j) * SLOT + 8192 + q * 1024);
            }
        wait_words<64>(p.ws + WS_OF + rt * 256, TAG, lane);
#pragma unroll
        for (int j = 0; j < 3; ++j)
#pragma unroll
            for (int i = 0; i < 2; ++i)
                dma_dev(vl16, p.ws + WS_O + (size_t)rt * 131072 + j * 8192 + (2 * hw + i) * 1024, ldsb + (2 + j) * SLOT + (2 * hw + i) * 1024);
    } else {
        const int al = (lane >> 4) & 1, m = lane & 15;
        const lptr ap = lds + L_H + (wid * H_CH + ((m & 1) ? 0 : H_CS) + al * H_SEG + ((m + 2) >> 1) + 4 * h) * 4;
        const lptr bp = lds + L_U + wid * U_CH + h * U_G + 16 * (127 - 16 * al - m);
        f32x16 c0 = {}, c1 = {}, c2 = {}, c3 = {};
#pragma unroll 2
        for (int kp = 0; kp < 16; ++kp) {
            bf16x8 a[4], b[4];
#pragma unroll
            for (int i = 0; i < 4; ++i) {
                const LAS u32* q = (const LAS u32*)(ap + (2 * i * H_SEG + 8 * kp) * 4);
                a[i] = __builtin_bit_cast(bf16x8, (u32x4){q[0], q[1], q[2], q[3]});
                b[i] = ldsb128(bp + 16 * kp - 512 * i);
            }
            c3 = MFMA32(a[0], b[3], c3); c2 = MFMA32(a[0], b[2], c2); c1 = MFMA32(a[0], b[1], c1); c0 = MFMA32(a[0], b[0], c0);
            c3 = MFMA32(a[1], b[2], c3); c2 = MFMA32(a[1], b[1], c2); c1 = MFMA32(a[1], b[0], c1);
            c3 = MFMA32(a[2], b[1], c3); c2 = MFMA32(a[2], b[0], c2);
            c3 = MFMA32(a[3], b[0], c3);
        }
        float y[8][4];
        auto fold = [&](const f32x16& c, int C) {
#pragma unroll
            for (int i = 0; i < 4; ++i) {
                auto x = __builtin_amdgcn_permlane16_swap(__float_as_uint(c[i]), __float_as_uint(c[i + 4]), false, false);
                auto r = __builtin_amdgcn_permlane16_swap(__float_as_uint(c[8 + i]), __float_as_uint(c[12 + i]), false, false);
                if (C == 0) y[0][i] = __uint_as_float(x[0]); else y[2 * C][i] += __uint_as_float(x[0]);
                y[2 * C + 1][i] = __uint_as_float(x[1]) + __uint_as_float(r[0]);
                if (C < 3) y[2 * C + 2][i] = __uint_as_float(r[1]);
            }
        };
        fold(c0, 0); fold(c1, 1); fold(c2, 2); fold(c3, 3);
        const int ch = 4 * bid + wid;
        const float dd = __uint_as_float((u32)p.ssmd[ch] << 16);
        const int tl = 16 * m + 8 * al + 4 * h;
        const char* zg = p.ws + WS_ZT + (size_t)ch * 4096;
#pragma unroll
        for (int c = 0; c < 8; ++c) {
            const int xr = 2044 - 256 * c - tl;
            const u32x2 uu = *(const LAS u32x2*)(lds + L_U + wid * U_CH + ((xr >> 3) & 1) * U_G + (xr >> 4) * 16 + (xr & 4) * 2);
            const float z0 = fmaf(bfhi(uu[1]), dd, y[c][0]), z1 = fmaf(bflo(uu[1]), dd, y[c][1]);
            const float z2 = fmaf(bfhi(uu[0]), dd, y[c][2]), z3 = fmaf(bflo(uu[0]), dd, y[c][3]);
            st8_dev((u32)(256 * c + tl) * 2, zg, u32x2{pk2(z0, z1), pk2(z2, z3)});
        }
        WAIT_VM0();
        if (lane == 0) st4_dev(p.ws + WS_ZF + ch * 4, TAG);
    }
    STAMP(6);

    f32x16 bacc = {};
    {
        WAIT_BAR(0);
        auto issue3 = [&](auto J) {
            constexpr int j = J.value;
#pragma unroll
            for (int i = 0; i < 3; ++i) {
                const u32 dst = ldsb + ((2 + j) % 6) * SLOT + (wid * 3 + i) * 1024;
                if (wid * 3 + i < 8) dma_dev(voC[i], sbC[i] + (size_t)j * stC[i], dst); else dma(voC[i], sbC[i] + (size_t)j * stC[i], dst);
            }
        };
        static_for<3, 6>(issue3);
        const int blk = (lane >> 4) & 1, q = (lane & 15) >> 2, pp = lane & 3;
        const int ch = 4 * hd + 2 * blk + (pp >> 1);
        wp0 = lds + 8192 + 256 * (4 * h + q) + 16 * (ch ^ ((q << 2) | h)) + 8 * (pp & 1);
        wp1 = lds + 8192 + 256 * (8 + 4 * h + q) + 16 * (ch ^ ((q << 2) | (2 + h))) + 8 * (pp & 1);
#pragma unroll
        for (int ks = 0; ks < 4; ++ks) xp[ks] = lds + (ks >> 1) * 4096 + rh * 2048 + (ks & 1) * 1024 + lane * 16;
        static_for<0, 16>([&](auto J) {
            constexpr int j = J.value;
            constexpr int last = j == 0 ? 5 : (j + 4 < 15 ? j + 4 : 15);
            constexpr int nz = j == 12 + 1 ? 0 : 0;
            if constexpr (j > 0) WAIT_BAR((j < 3 ? 3 * (last - 2) : 3 * (last - j)) + nz + (j > 12 ? 2 : 0));
            if constexpr (j >= 1 && j + 5 <= 15) issue3(std::integral_constant<int, j + 5>{});
            if constexpr (j == 12) {
                wait_words<128>(p.ws + WS_ZF + hg * 512, TAG, lane);
#pragma unroll
                for (int i = 0; i < 2; ++i) {
                    const int row = 8 * (2 * wid + i) + (lane >> 3);
                    dma_dev(row * 4096 + (((lane & 7) ^ (((row >> 1) & 1) << 2)) * 16), p.ws + WS_ZT + (size_t)hg * 128 * 4096 + rt * 128,
                            ldsb + ((2 + 11) % 6) * SLOT + (2 * wid + i) * 1024);
                }
            }
            gemm_stage<false>(bacc, wp0, wp1, xp, ((2 + j) % 6) * SLOT);
        });
    }
    STAMP(7);
    {
        const int t = 64 * rt + 32 * rh + r32, cb = 128 * hg + 32 * hd;
        u32x2 xv[4];
#pragma unroll
        for (int i = 0; i < 4; ++i) xv[i] = *(const u32x2*)(p.x + (size_t)t * D + cb + 8 * i + 4 * h);
        const float g = __uint_as_float(ld4_dev(p.ws + WS_G + t * 4));
        WAIT_BAR(0);
        const int q = (lane & 15) >> 2, pp = lane & 3, blk = (lane >> 4) & 1;
        u32x2 ov[4];
#pragma unroll
        for (int i = 0; i < 4; ++i) {
            const int row = 32 * hd + 8 * i + 4 * h + q;
            const int chunk = 4 * rh + 2 * blk + (pp >> 1);
            const s16x4 z = ldstr(lds + ((2 + 11) % 6) * SLOT + row * 128 + 16 * (chunk ^ (((row >> 1) & 1) << 2)) + 8 * (pp & 1));
            const u32x2 zz = __builtin_bit_cast(u32x2, z);
            const float g1 = 1.f - g;
            const float o0 = fmaf(g * bflo(xv[i][0]), bflo(zz[0]), g1 * bacc[4 * i]);
            const float o1 = fmaf(g * bfhi(xv[i][0]), bfhi(zz[0]), g1 * bacc[4 * i + 1]);
            const float o2 = fmaf(g * bflo(xv[i][1]), bflo(zz[1]), g1 * bacc[4 * i + 2]);
            const float o3 = fmaf(g * bfhi(xv[i][1]), bfhi(zz[1]), g1 * bacc[4 * i + 3]);
            ov[i] = u32x2{pk2(o0, o1), pk2(o2, o3)};
        }
#pragma unroll
        for (int i = 0; i < 4; i += 2) {
            auto a = __builtin_amdgcn_permlane32_swap(ov[i][0], ov[i + 1][0], false, false);
            auto b = __builtin_amdgcn_permlane32_swap(ov[i][1], ov[i + 1][1], false, false);
            { const u32x4 w_ = u32x4{a[0], b[0], a[1], b[1]}; const u32 o_ = (u32)(t * D + cb + 8 * i + 8 * h) * 2;
              asm volatile("global_store_dwordx4 %0, %1, %2 nt\n\ts_nop 1" ::"v"(o_), "v"(w_), "s"(p.out) : "memory"); }
        }
    }
    STAMP(8);
}

extern "C" void kernel_launch(void* const* d_in, const int* in_sizes, int n_in, void* d_out, int out_size, void* d_ws, size_t ws_size, hipStream_t stream) {
    Params p;
    p.x = (const u16*)d_in[0]; p.k = (const u16*)d_in[1]; p.v = (const u16*)d_in[2]; p.sk = (const u16*)d_in[3];
    p.ssmk = (const u16*)d_in[4]; p.ssmd = (const u16*)d_in[5]; p.wq = (const u16*)d_in[7]; p.wk = (const u16*)d_in[8];
    p.wv = (const u16*)d_in[9]; p.wo = (const u16*)d_in[10]; p.gw = (const u16*)d_in[11];
    p.out = (u16*)d_out; p.ws = (char*)d_ws;
    (void)hipFuncSetAttribute((const void*)fused_kernel, hipFuncAttributeMaxDynamicSharedMemorySize, LDS_BYTES);
    fused_kernel<<<256, NT, LDS_BYTES, stream>>>(p);
}
```

```cpp
#include <hip/hip_runtime.h>
#include <stdint.h>

typedef unsigned short u16;
typedef unsigned int u32;
typedef unsigned long long u64;
using bf16x8 = __attribute__((ext_vector_type(8))) short;
using f32x16 = __attribute__((ext_vector_type(16))) float;
using f32x4 = __attribute__((ext_vector_type(4))) float;
using f32x2 = __attribute__((ext_vector_type(2))) float;
using bf16x2 = __attribute__((ext_vector_type(2))) __bf16;
using u32x4 = __attribute__((ext_vector_type(4))) unsigned;
using u32x2 = __attribute__((ext_vector_type(2))) unsigned;
using u32x16 = __attribute__((ext_vector_type(16))) unsigned;
#define LAS __attribute__((address_space(3)))
#define GAS __attribute__((address_space(1)))
typedef LAS char* lptr;

constexpr int S = 2048, D = 1024;
constexpr int NT = 512;
constexpr int CODE_LINES = 308;

constexpr size_t WS_EP = 0;
constexpr size_t WS_KVF = 4096;
constexpr size_t WS_UF = 8192;
constexpr size_t WS_G = 32768;
constexpr size_t WS_OF = 40960;
constexpr size_t WS_ZF = 49152;
constexpr size_t WS_KV = 65536;
constexpr size_t WS_UT = 1u << 20;
constexpr size_t WS_O = 5u << 20;
constexpr size_t WS_ZT = 9u << 20;

constexpr int LDS_BYTES = 163840;
constexpr int L_XR = 0;
constexpr int L_QR = 65536, QSLOT = 20480;
constexpr int L_WKV = 126976;
constexpr int L_KV = 0;
constexpr int L_H = 108032, H_CS = 1040, H_CH = 2080;
constexpr int L_U = 141312, U_G = 2304, U_CH = 4608;
constexpr int L_X = 16384, L_OUT = 32768, L_Z = L_U;
constexpr int L_MISC = 159744;
constexpr int M_TAG = L_MISC;
constexpr int M_FULL = L_MISC + 16;
constexpr int M_JOIN = L_MISC + 32;
constexpr int M_OJ = L_MISC + 48;
constexpr int M_GO = L_MISC + 52;
constexpr int M_CRDY = L_MISC + 56;
constexpr int M_CR = L_MISC + 60;
constexpr int M_ZJ = L_MISC + 64;
constexpr int M_ZOK = L_MISC + 68;
constexpr int M_K2 = L_MISC + 80;
constexpr int M_G = L_MISC + 256;
constexpr int M_WRDY = L_MISC + 512;
constexpr int M_FREE = L_MISC + 1024;
constexpr int M_GW = L_MISC + 2048;

constexpr float QSCALE = 0.17677669529663687f * 1.4426950408889634f;
constexpr float SAFE_T2 = 64.f * 64.f;
constexpr float SAFE_V = 1099511627776.f;
constexpr u32 SPIN_CAP = 1u << 17;
constexpr u32 spin_cap(u32 sleep) { return (1u << 30) / (64u * sleep + 128u); }

struct Params {
    const u16 *x, *k, *v, *sk, *ssmk, *ssmd, *wq, *wk, *wv, *wo, *gw;
    u16* out;
    char* ws;
};

#define MFMA32(a, b, c) __builtin_amdgcn_mfma_f32_32x32x16_bf16(a, b, c, 0, 0, 0)
#define WAIT_BAR(N) asm volatile("s_waitcnt vmcnt(%c0) lgkmcnt(0)\n\ts_barrier" ::"i"(N) : "memory")
#define BAR() asm volatile("s_waitcnt lgkmcnt(0)\n\ts_barrier" ::: "memory")
#define WAIT_VM0() asm volatile("s_waitcnt vmcnt(0)" ::: "memory")

__device__ __forceinline__ u32 rfl(u32 v) { return __builtin_amdgcn_readfirstlane(v); }
__device__ __forceinline__ bool vm_idle() { return rfl(__builtin_amdgcn_s_getreg(7 | (0 << 6) | (3 << 11))) == 0; }
__device__ __forceinline__ float bflo(u32 v) { return __uint_as_float(v << 16); }
__device__ __forceinline__ float bfhi(u32 v) { return __uint_as_float(v & 0xffff0000u); }
__device__ __forceinline__ u32 pk2(float lo, float hi) {
    f32x2 f = {lo, hi};
    return __builtin_bit_cast(u32, __builtin_convertvector(f, bf16x2));
}
__device__ __forceinline__ void dma(u32 voff, const void* sbase, u32 ldsaddr) {
    asm volatile("s_mov_b32 m0, %2\n\ts_nop 0\n\tglobal_load_lds_dwordx4 %0, %1" ::"v"(voff), "s"(sbase), "s"(ldsaddr) : "memory");
}
__device__ __forceinline__ void dma_dev(u32 voff, const void* sbase, u32 ldsaddr) {
    asm volatile("s_mov_b32 m0, %2\n\ts_nop 0\n\tglobal_load_lds_dwordx4 %0, %1 sc1" ::"v"(voff), "s"(sbase), "s"(ldsaddr) : "memory");
}
__device__ __forceinline__ void st16_dev(u32 voff, const void* sbase, u32x4 v) {
    asm volatile("global_store_dwordx4 %0, %1, %2 sc1\n\ts_nop 1" ::"v"(voff), "v"(v), "s"(sbase) : "memory");
}
__device__ __forceinline__ void st8_dev(u32 voff, const void* sbase, u32x2 v) {
    asm volatile("global_store_dwordx2 %0, %1, %2 sc1" ::"v"(voff), "v"(v), "s"(sbase) : "memory");
}
__device__ __forceinline__ void st8g_dev(void* ptr, u64 v) { __hip_atomic_store((GAS u64*)ptr, v, __ATOMIC_RELAXED, __HIP_MEMORY_SCOPE_AGENT); }
__device__ __forceinline__ u64 ld8_dev(const void* ptr) { return __hip_atomic_load((GAS const u64*)ptr, __ATOMIC_RELAXED, __HIP_MEMORY_SCOPE_AGENT); }
__device__ __forceinline__ void ld16_issue(u32x4& r, u32 voff, const void* sbase) {
    asm volatile("global_load_dwordx4 %0, %1, %2 sc1" : "=v"(r) : "v"(voff), "s"(sbase) : "memory");
}

__device__ __forceinline__ void ld8_issue(u32x2& r, u32 voff, const void* sbase) {
    asm volatile("global_load_dwordx2 %0, %1, %2 sc1" : "=v"(r) : "v"(voff), "s"(sbase) : "memory");
}

__device__ __forceinline__ bf16x8 ldsb128(lptr p) { return *(const LAS bf16x8*)p; }
__device__ __forceinline__ float swap32sum(float v) {
    auto r = __builtin_amdgcn_permlane32_swap(__float_as_uint(v), __float_as_uint(v), false, false);
    return __uint_as_float(r[0]) + __uint_as_float(r[1]);
}
__device__ __forceinline__ float swap32max(float v) {
    auto r = __builtin_amdgcn_permlane32_swap(__float_as_uint(v), __float_as_uint(v), false, false);
    return fmaxf(__uint_as_float(r[0]), __uint_as_float(r[1]));
}
#define DPP_STEP(op, v, ctrl, rmask) v = op(v, __builtin_bit_cast(decltype(v), __builtin_amdgcn_update_dpp(0, __builtin_bit_cast(int, v), ctrl, rmask, 0xf, true)))
template <int SLP>
__device__ __forceinline__ void lds_wait(lptr word, u32 n) {
#pragma unroll 1
    for (u32 spins = 0; spins < spin_cap(SLP); ++spins) {
        if (rfl(*(volatile LAS u32*)word) >= n) break;
        __builtin_amdgcn_s_sleep(SLP);
    }
    asm volatile("" ::: "memory");
}
__device__ __forceinline__ void lds_post(lptr word, int lane) {
    asm volatile("s_waitcnt vmcnt(0) lgkmcnt(0)" ::: "memory");
    if (lane == 0) __hip_atomic_fetch_add((LAS u32*)word, 1u, __ATOMIC_RELAXED, __HIP_MEMORY_SCOPE_WORKGROUP);
}
__device__ __forceinline__ u32 umax2(u32 a, u32 b) { return max(a, b); }
__device__ __forceinline__ u32 wave_umax(u32 v) {
    DPP_STEP(umax2, v, 0x111, 0xf); DPP_STEP(umax2, v, 0x112, 0xf); DPP_STEP(umax2, v, 0x114, 0xf); DPP_STEP(umax2, v, 0x118, 0xf);
    DPP_STEP(umax2, v, 0x142, 0xa); DPP_STEP(umax2, v, 0x143, 0xc);
    return __builtin_amdgcn_readlane(v, 63);
}
__device__ __forceinline__ float swap16sum(float v) {
    auto r = __builtin_amdgcn_permlane16_swap(__float_as_uint(v), __float_as_uint(v), false, false);
    return __uint_as_float(r[0]) + __uint_as_float(r[1]);
}
__device__ __forceinline__ float dot2(u32 a, u32 b, float c) {
    return __builtin_amdgcn_fdot2_f32_bf16(__builtin_bit_cast(bf16x2, a), __builtin_bit_cast(bf16x2, b), c, false);
}
__device__ __forceinline__ float mul1(float a, float b) { float r = a * b; asm("" : "+v"(r)); return r; }
__device__ __forceinline__ float add1(float a, float b) { float r = a + b; asm("" : "+v"(r)); return r; }
__device__ __forceinline__ bf16x8 pack8(const f32x16& a, int o) {
    return __builtin_bit_cast(bf16x8, (u32x4){pk2(a[o], a[o + 1]), pk2(a[o + 2], a[o + 3]), pk2(a[o + 4], a[o + 5]), pk2(a[o + 6], a[o + 7])});
}
__device__ __forceinline__ u32x4 pack8s(const f32x16& a, int o, float s) {
    return u32x4{pk2(mul1(a[o], s), mul1(a[o + 1], s)), pk2(mul1(a[o + 2], s), mul1(a[o + 3], s)), pk2(mul1(a[o + 4], s), mul1(a[o + 5], s)), pk2(mul1(a[o + 6], s), mul1(a[o + 7], s))};
}

template <int SLP>
__device__ __forceinline__ void wait_tags32(const char* base, u64 tag, int lane) {
#pragma unroll 1
    for (u32 spins = 0; spins < SPIN_CAP; ++spins) {
        if (__all(ld8_dev(base + (lane & 31) * 8) == tag)) break;
        __builtin_amdgcn_s_sleep(SLP);
    }
    __builtin_amdgcn_fence(__ATOMIC_ACQUIRE, "wavefront");
}

__global__ __launch_bounds__(NT, 2) void fused_kernel(Params p) {
    extern __shared__ __attribute__((aligned(16))) char smem[];
    const lptr lds = (lptr)smem;
    const u32 ldsb = (u32)(size_t)smem;
    const int tid = threadIdx.x, lane = tid & 63;
    const int wid = rfl(tid >> 6);
    const int bid = blockIdx.x;
    const int xcd = bid & 7, ix = bid >> 3;
    u32 pfc = 0;
    if (wid == 0 && lane * 32 + ix < CODE_LINES) {
        u64 pc0;
        asm volatile("s_getpc_b64 %0" : "=s"(pc0));
        asm volatile("global_load_dword %0, %1, %2" : "=v"(pfc) : "v"((lane * 32 + ix) * 128), "s"(pc0 & ~(u64)255) : "memory");
    }
    const int rt = (xcd >> 1) * 8 + (ix >> 2), hg = (xcd & 1) * 4 + (ix & 3);
    const int r32 = lane & 31, h = lane >> 5;
    const int cg = hg >> 1;
    const u32 vl16 = lane * 16;
    const u32 voP = (lane >> 2) * 2048 + ((lane & 3) ^ ((lane >> 3) & 2)) * 16;
    const int par = hg & 1;
    const int rot = (4 * (xcd >> 1) + 8 * (xcd & 1)) & 15;
    const int rot3 = (2 * (rt & 7) + 4 * (hg & 3) + ((xcd >> 1) & 1)) & 15;

    auto store_rows = [&]() {
#pragma unroll
        for (int i = 0; i < 2; ++i) {
            const int row = 8 * wid + 4 * i + (lane >> 4);
            const u32x4 w = *(const LAS u32x4*)(lds + L_OUT + row * 256 + (((lane & 15) ^ (row & 15)) * 16));
            asm volatile("global_store_dwordx4 %0, %1, %2 sc1 nt\n\ts_nop 1" ::"v"(row * 2048 + (lane & 15) * 16), "v"(w), "s"((char*)p.out + (size_t)rt * 131072 + hg * 256) : "memory");
        }
    };
    if (wid >= 4) {
        const int hw = wid - 4;
        const u32 voW = (4 * hw + (lane >> 4)) * 2048 + (((lane & 15) ^ (((lane >> 4) << 2) | hw)) * 16);
        const u32 voX = (8 * hw + (lane >> 3)) * 2048 + (((lane & 7) ^ ((4 * hw + (lane >> 4)) & 7)) * 16);
        const char* kvg = p.ws + WS_KV;
        {
            const u32 voA = (lane >> 1) * 64 + (lane & 1) * 16;
            const char* xa = (const char*)p.x + (size_t)(64 * rt + 32 * par) * 2048;
            const char* xb = (const char*)p.x + (size_t)(64 * rt + 32 * (1 - par)) * 2048;
            const char* wa = (const char*)(cg < 2 ? p.wk : p.wv) + (cg & 1) * 32;
            const char* wb = (const char*)p.wq + hg * 256;
            auto issue_a = [&](int q) {
#pragma unroll
                for (int i = 0; i < 4; ++i) dma(voX, xa + ((rot + 4 * q + i) & 15) * 128, ldsb + L_XR + (4 * q + i) * 4096 + hw * 1024);
                const char* w = wa + ((rot + 4 * q + hw) & 15) * 4096;
                const u32 d = ldsb + L_WKV + (3 - q) * 8192 + hw * 2048;
                dma(voA, w, d); dma(voA, w + 2048, d + 1024);
            };
            auto issue_q = [&](int j) {
                const int kc = (rot + j) & 15;
                const u32 d = ldsb + L_QR + (j & 3) * QSLOT + hw * 1024;
                const char* w = wb + (size_t)kc * 64 * 2048;
                dma(voX, xb + kc * 128, d);
                dma(voW, w, d + 4096); dma(voW, w + 16 * 2048, d + 8192); dma(voW, w + 32 * 2048, d + 12288); dma(voW, w + 48 * 2048, d + 16384);
            };
            if (hw == 0) {
#pragma unroll
                for (int i = 0; i < 2; ++i) dma(((rot + 8 * i + (lane >> 3)) & 15) * 128 + (lane & 7) * 16, p.gw, ldsb + M_GW + i * 1024);
            }
            issue_a(0); issue_a(1); issue_a(2);
            WAIT_BAR(12); issue_a(3);
            WAIT_BAR(12); issue_q(0);
            WAIT_BAR(11); issue_q(1);
            WAIT_BAR(10); issue_q(2);
            {
                const u32 d0 = ldsb + L_QR + hw * 1024;
                u32 kc = rot + 2, dl = d0 + 2 * QSLOT, nn = 12, tt;
    asm volatile(
        "LOOP_%=:\n\t"
        "s_add_u32 %[kc], %[kc], 1\n\t"
        "s_and_b32 %[kc], %[kc], 15\n\t"
        "s_lshl_b32 %[t], %[kc], 7\n\t"
        "s_add_u32 s92, %[xlo], %[t]\n\t"
        "s_addc_u32 s93, %[xhi], 0\n\t"
        "s_lshl_b32 %[t], %[kc], 17\n\t"
        "s_add_u32 s94, %[wlo], %[t]\n\t"
        "s_addc_u32 s95, %[whi], 0\n\t"
        "s_add_u32 %[d], %[d], %[dq]\n\t"
        "s_cmp_eq_u32 %[d], %[dend]\n\t"
        "s_cselect_b32 %[d], %[d0], %[d]\n\t"
        "s_mov_b32 m0, %[d]\n\t"
        "s_waitcnt vmcnt(10)\n\t"
        "s_barrier\n\t"
        "global_load_lds_dwordx4 %[vx], s[92:93]\n\t"
        "s_add_u32 m0, %[d], 4096\n\t"
        "s_nop 0\n\t"
        "global_load_lds_dwordx4 %[vw0], s[94:95]\n\t"
        "s_add_u32 m0, %[d], 8192\n\t"
        "s_nop 0\n\t"
        "global_load_lds_dwordx4 %[vw1], s[94:95]\n\t"
        "s_add_u32 m0, %[d], 12288\n\t"
        "s_nop 0\n\t"
        "global_load_lds_dwordx4 %[vw2], s[94:95]\n\t"
        "s_add_u32 m0, %[d], 16384\n\t"
        "s_nop 0\n\t"
        "global_load_lds_dwordx4 %[vw3], s[94:95]\n\t"
        "s_sub_u32 %[n], %[n], 1\n\t"
        "s_cbranch_scc0 LOOP_%=\n\t"
        : [kc] "+&s"(kc), [d] "+&s"(dl), [n] "+&s"(nn), [t] "=&s"(tt)
        : [vx] "v"(voX), [vw0] "v"(voW), [vw1] "v"(voW + 32768), [vw2] "v"(voW + 65536), [vw3] "v"(voW + 98304),
          [xlo] "s"((u32)(u64)xb), [xhi] "s"((u32)((u64)xb >> 32)), [wlo] "s"((u32)(u64)wb), [whi] "s"((u32)((u64)wb >> 32)),
          [dq] "s"((u32)QSLOT), [d0] "s"(d0), [dend] "s"(d0 + 4 * QSLOT)
        : "memory", "scc", "s92", "s93", "s94", "s95");
            }
            WAIT_BAR(10);
#pragma unroll
            for (int i = 0; i < 8; ++i) dma_dev(vl16, kvg + (8 * hw + i) * 1024, ldsb + L_KV + (8 * hw + i) * 1024);
            u32x4 f0, f1, f2, f3;
            if (hw == 3) {
                ld16_issue(f0, vl16, p.ws + WS_KVF); ld16_issue(f1, vl16, p.ws + WS_KVF + 1024);
                ld16_issue(f2, vl16, p.ws + WS_KVF + 2048); ld16_issue(f3, vl16, p.ws + WS_KVF + 3072);
                WAIT_BAR(17);
                WAIT_BAR(12);
                asm volatile("s_waitcnt vmcnt(0)" : "+v"(f0), "+v"(f1), "+v"(f2), "+v"(f3)::"memory");
                const u32 val = max(max(f0[1], f1[1]), max(f2[1], f3[1]));
                const int c = (lane >> 1) & 3;
                const u32 k0 = wave_umax(c == 0 ? val : 0u), k1 = wave_umax(c == 1 ? val : 0u), vm = wave_umax(c >= 2 ? val : 0u);
                const bool vok = __uint_as_float(vm) <= SAFE_V && __uint_as_float(vm) >= 1.f / SAFE_V;
                const float k2 = vok ? __uint_as_float(k0) + __uint_as_float(k1) : __builtin_inff();
                if (lane == 0) *(LAS float*)(lds + M_K2) = k2;
            } else {
                WAIT_BAR(13);
                WAIT_BAR(8);
            }
            WAIT_BAR(0);
        }
        const u32x2 tg2 = *(const LAS u32x2*)(lds + M_TAG);
        const u64 tag = ((u64)rfl(tg2[1]) << 32) | rfl(tg2[0]);

        u32x4 xt[4];
#pragma unroll
        for (int i = 0; i < 4; ++i) xt[i] = *(const u32x4*)(p.x + (size_t)(64 * rt + 4 * (4 * hw + i) + (lane >> 4)) * D + 128 * hg + (lane & 15) * 8);
        u32 dsk = p.ssmd[4 * bid + hw];
        const __amdgpu_buffer_rsrc_t rk = __builtin_amdgcn_make_buffer_rsrc((void*)p.k, 0, S * D * 2, 0x00020000);
        const __amdgpu_buffer_rsrc_t rv = __builtin_amdgcn_make_buffer_rsrc((void*)p.v, 0, S * D * 2, 0x00020000);
        const int cp = lane & 7, tp = 64 * rt + 8 * (lane >> 3);
        const u32 vo = tp * 2048 + (128 * hg + 2 * cp) * 2, voh = tp ? vo - 3 * 2048 : 0x7fff0000u;
        const u32 uo = (u32)(2 * cp * 4096 + (2040 - tp) * 2);
        const int g = lane >> 4, m = lane & 15;
        const u32 ap = ldsb + L_H + (hw * H_CH + ((m & 1) ? 0 : H_CS) + ((m + 2) >> 1) + 4 * g) * 4;
        const u32 bp = ldsb + L_U + hw * U_CH + (g & 1) * U_G + 16 * (15 - m + (g >> 1));
        auto stage_w = [&](int st, u32 dst) {
            const char* src = (const char*)p.wo + hg * 256 + (size_t)((rot3 + st) & 15) * 64 * 2048;
#pragma unroll
            for (int i = 0; i < 16; ++i) dma(voP, src + (i >> 2) * 64 + (i & 3) * 16 * 2048, ldsb + dst + i * 1024);
        };
        auto wait_free = [&](int n) {
#pragma unroll 1
            for (u32 spins = 0; spins < spin_cap(8); ++spins) {
                const u32 f = *(volatile LAS u32*)(lds + M_FREE + (lane & 3) * 256);
                if (__all((int)f >= n)) break;
                __builtin_amdgcn_s_sleep(8);
            }
        };
        auto prep = [&](int task) {
            const char* skb = (const char*)p.sk + (128 * hg + 16 * task) * 8;
            const char* ub = p.ws + WS_UT + (size_t)(128 * hg + 16 * task) * 4096;
    asm volatile(
        "global_load_dwordx4 v[40:43], %[so], %[sk]\n\t"
        "buffer_load_dword v20, %[voh], %[rk], 0 offen offset:0\n\t"
        "buffer_load_dword v21, %[voh], %[rk], 0 offen offset:2048\n\t"
        "buffer_load_dword v22, %[voh], %[rk], %[c4] offen offset:0\n\t"
        "buffer_load_dword v23, %[vo], %[rk], 0 offen offset:0\n\t"
        "buffer_load_dword v24, %[vo], %[rk], 0 offen offset:2048\n\t"
        "buffer_load_dword v25, %[vo], %[rk], %[c4] offen offset:0\n\t"
        "buffer_load_dword v26, %[vo], %[rk], %[c4] offen offset:2048\n\t"
        "buffer_load_dword v27, %[vo], %[rk], %[c8] offen offset:0\n\t"
        "buffer_load_dword v28, %[vo], %[rk], %[c8] offen offset:2048\n\t"
        "buffer_load_dword v29, %[vo], %[rk], %[c12] offen offset:0\n\t"
        "buffer_load_dword v30, %[vo], %[rk], %[c12] offen offset:2048\n\t"
        "buffer_load_dword v32, %[vo], %[rv], 0 offen offset:0\n\t"
        "buffer_load_dword v33, %[vo], %[rv], 0 offen offset:2048\n\t"
        "buffer_load_dword v34, %[vo], %[rv], %[c4] offen offset:0\n\t"
        "buffer_load_dword v35, %[vo], %[rv], %[c4] offen offset:2048\n\t"
        "buffer_load_dword v36, %[vo], %[rv], %[c8] offen offset:0\n\t"
        "buffer_load_dword v37, %[vo], %[rv], %[c8] offen offset:2048\n\t"
        "buffer_load_dword v38, %[vo], %[rv], %[c12] offen offset:0\n\t"
        "buffer_load_dword v39, %[vo], %[rv], %[c12] offen offset:2048\n\t"
        "s_waitcnt vmcnt(8)\n\t"
        "s_setprio 1\n\t"
        "v_perm_b32 v44, v20, v21, %[slo]\n\t"
        "v_perm_b32 v54, v20, v21, %[shi]\n\t"
        "v_perm_b32 v45, v21, v22, %[slo]\n\t"
        "v_perm_b32 v55, v21, v22, %[shi]\n\t"
        "v_perm_b32 v46, v22, v23, %[slo]\n\t"
        "v_perm_b32 v56, v22, v23, %[shi]\n\t"
        "v_perm_b32 v47, v23, v24, %[slo]\n\t"
        "v_perm_b32 v57, v23, v24, %[shi]\n\t"
        "v_perm_b32 v48, v24, v25, %[slo]\n\t"
        "v_perm_b32 v58, v24, v25, %[shi]\n\t"
        "v_perm_b32 v49, v25, v26, %[slo]\n\t"
        "v_perm_b32 v59, v25, v26, %[shi]\n\t"
        "v_perm_b32 v50, v26, v27, %[slo]\n\t"
        "v_perm_b32 v60, v26, v27, %[shi]\n\t"
        "v_perm_b32 v51, v27, v28, %[slo]\n\t"
        "v_perm_b32 v61, v27, v28, %[shi]\n\t"
        "v_perm_b32 v52, v28, v29, %[slo]\n\t"
        "v_perm_b32 v62, v28, v29, %[shi]\n\t"
        "v_perm_b32 v53, v29, v30, %[slo]\n\t"
        "v_perm_b32 v63, v29, v30, %[shi]\n\t"
        "v_dot2_f32_bf16 v64, v44, v41, 0\n\t"
        "v_dot2_f32_bf16 v72, v54, v43, 0\n\t"
        "v_dot2_f32_bf16 v65, v45, v41, 0\n\t"
        "v_dot2_f32_bf16 v73, v55, v43, 0\n\t"
        "v_dot2_f32_bf16 v66, v46, v41, 0\n\t"
        "v_dot2_f32_bf16 v74, v56, v43, 0\n\t"
        "v_dot2_f32_bf16 v67, v47, v41, 0\n\t"
        "v_dot2_f32_bf16 v75, v57, v43, 0\n\t"
        "v_dot2_f32_bf16 v68, v48, v41, 0\n\t"
        "v_dot2_f32_bf16 v76, v58, v43, 0\n\t"
        "v_dot2_f32_bf16 v69, v49, v41, 0\n\t"
        "v_dot2_f32_bf16 v77, v59, v43, 0\n\t"
        "v_dot2_f32_bf16 v70, v50, v41, 0\n\t"
        "v_dot2_f32_bf16 v78, v60, v43, 0\n\t"
        "v_dot2_f32_bf16 v71, v51, v41, 0\n\t"
        "v_dot2_f32_bf16 v79, v61, v43, 0\n\t"
        "v_dot2_f32_bf16 v64, v46, v40, v64\n\t"
        "v_dot2_f32_bf16 v72, v56, v42, v72\n\t"
        "v_dot2_f32_bf16 v65, v47, v40, v65\n\t"
        "v_dot2_f32_bf16 v73, v57, v42, v73\n\t"
        "v_dot2_f32_bf16 v66, v48, v40, v66\n\t"
        "v_dot2_f32_bf16 v74, v58, v42, v74\n\t"
        "v_dot2_f32_bf16 v67, v49, v40, v67\n\t"
        "v_dot2_f32_bf16 v75, v59, v42, v75\n\t"
        "v_dot2_f32_bf16 v68, v50, v40, v68\n\t"
        "v_dot2_f32_bf16 v76, v60, v42, v76\n\t"
        "v_dot2_f32_bf16 v69, v51, v40, v69\n\t"
        "v_dot2_f32_bf16 v77, v61, v42, v77\n\t"
        "v_dot2_f32_bf16 v70, v52, v40, v70\n\t"
        "v_dot2_f32_bf16 v78, v62, v42, v78\n\t"
        "v_dot2_f32_bf16 v71, v53, v40, v71\n\t"
        "v_dot2_f32_bf16 v79, v63, v42, v79\n\t"
        "s_waitcnt vmcnt(0)\n\t"
        "v_lshlrev_b32 v20, 16, v32\n\t"
        "v_and_b32 v32, 0xffff0000, v32\n\t"
        "v_lshlrev_b32 v21, 16, v33\n\t"
        "v_and_b32 v33, 0xffff0000, v33\n\t"
        "v_lshlrev_b32 v22, 16, v34\n\t"
        "v_and_b32 v34, 0xffff0000, v34\n\t"
        "v_lshlrev_b32 v23, 16, v35\n\t"
        "v_and_b32 v35, 0xffff0000, v35\n\t"
        "v_lshlrev_b32 v24, 16, v36\n\t"
        "v_and_b32 v36, 0xffff0000, v36\n\t"
        "v_lshlrev_b32 v25, 16, v37\n\t"
        "v_and_b32 v37, 0xffff0000, v37\n\t"
        "v_lshlrev_b32 v26, 16, v38\n\t"
        "v_and_b32 v38, 0xffff0000, v38\n\t"
        "v_lshlrev_b32 v27, 16, v39\n\t"
        "v_and_b32 v39, 0xffff0000, v39\n\t"
        "v_mul_f32 v64, v64, v20\n\t"
        "v_mul_f32 v72, v72, v32\n\t"
        "v_mul_f32 v65, v65, v21\n\t"
        "v_mul_f32 v73, v73, v33\n\t"
        "v_mul_f32 v66, v66, v22\n\t"
        "v_mul_f32 v74, v74, v34\n\t"
        "v_mul_f32 v67, v67, v23\n\t"
        "v_mul_f32 v75, v75, v35\n\t"
        "v_mul_f32 v68, v68, v24\n\t"
        "v_mul_f32 v76, v76, v36\n\t"
        "v_mul_f32 v69, v69, v25\n\t"
        "v_mul_f32 v77, v77, v37\n\t"
        "v_mul_f32 v70, v70, v26\n\t"
        "v_mul_f32 v78, v78, v38\n\t"
        "v_mul_f32 v71, v71, v27\n\t"
        "v_mul_f32 v79, v79, v39\n\t"
        "v_cvt_pk_bf16_f32 v80, v71, v70\n\t"
        "v_cvt_pk_bf16_f32 v81, v69, v68\n\t"
        "v_cvt_pk_bf16_f32 v82, v67, v66\n\t"
        "v_cvt_pk_bf16_f32 v83, v65, v64\n\t"
        "v_cvt_pk_bf16_f32 v84, v79, v78\n\t"
        "v_cvt_pk_bf16_f32 v85, v77, v76\n\t"
        "v_cvt_pk_bf16_f32 v86, v75, v74\n\t"
        "v_cvt_pk_bf16_f32 v87, v73, v72\n\t"
        "global_store_dwordx4 %[uo], v[80:83], %[ub0] sc1\n\t"
        "global_store_dwordx4 %[uo], v[84:87], %[ub1] sc1\n\t"
        "s_nop 1\n\t"
        :: [vo] "v"(vo + task * 32), [voh] "v"(voh + task * 32), [rk] "s"(rk), [rv] "s"(rv), [c4] "s"(4096u), [c8] "s"(8192u), [c12] "s"(12288u),
           [so] "v"(cp * 16), [sk] "s"(skb), [slo] "s"(0x05040100u), [shi] "s"(0x07060302u), [uo] "v"(uo), [ub0] "s"(ub), [ub1] "s"(ub + 4096)
        : "memory", "v20", "v21", "v22", "v23", "v24", "v25", "v26", "v27", "v28", "v29", "v30", "v31", "v32", "v33", "v34", "v35", "v36", "v37", "v38", "v39", "v40", "v41", "v42", "v43", "v44", "v45", "v46", "v47", "v48", "v49", "v50", "v51", "v52", "v53", "v54", "v55", "v56", "v57", "v58", "v59", "v60", "v61", "v62", "v63", "v64", "v65", "v66", "v67", "v68", "v69", "v70", "v71", "v72", "v73", "v74", "v75", "v76", "v77", "v78", "v79", "v80", "v81", "v82", "v83", "v84", "v85", "v86", "v87");
        };
        if (hw == 0) {
#pragma unroll 1
            for (int t = 2; t < 16; ++t) {
                wait_free(t - 3);
#pragma unroll
                for (int i = 0; i < 16; ++i) dma_dev(vl16, kvg + t * 16384 + i * 1024, ldsb + L_KV + (t & 3) * 16384 + i * 1024);
                WAIT_VM0();
                *(volatile LAS u32*)(lds + M_FULL) = t == 15 ? 17u : (u32)(t + 1);
            }
            __builtin_amdgcn_s_setprio(1);
        } else {
            __builtin_amdgcn_s_setprio(1);
            lds_wait<8>(lds + M_FULL, 4u);
            {
                const int n = (hw - 1) * 64 + lane;
                u32 t0 = __builtin_amdgcn_raw_buffer_load_b32(rk, n < 134 ? (u32)((64 * rt - 3 + (n >> 1)) * 2048 + hg * 256 + (n & 1) * 128) : 0x7fff0000u, 0, 0);
                asm volatile("" : "+v"(t0));
                lds_wait<8>(lds + M_FULL, 6u);
                u32 t1 = __builtin_amdgcn_raw_buffer_load_b32(rv, n < 128 ? (u32)((64 * rt + (n >> 1)) * 2048 + hg * 256 + (n & 1) * 128) : 0x7fff0000u, 0, 0);
                asm volatile("" : "+v"(t1));
                lds_wait<8>(lds + M_FULL, 7u);
            }
#pragma unroll 1
            for (int task = 3 - hw; task < 8; task += 3) prep(task);
            WAIT_VM0();
            if (lane < (hw > 1 ? 3 : 2)) st8g_dev(p.ws + WS_UF + ((hg * 8 + 3 - hw + 3 * lane) * 32 + rt) * 8, tag);
            if (hw == 1) {
                lds_wait<40>(lds + M_FULL, 10u);
                const u32 vo0 = lane ? vl16 : 16u;
#pragma unroll
                for (int j = 0; j < 8; ++j)
#pragma unroll
                    for (int i = 0; i < 4; ++i)
                        dma(i ? vl16 : vo0, (const char*)p.ssmk + (size_t)(4 * bid + (j & 3)) * 4096 + i * 1024 - (j >> 2) * 2,
                            ldsb + L_H + ((j & 3) * H_CH + (j >> 2) * H_CS) * 4 + 32 + i * 1024);
                const int j = (lane >> 2) & 7, c = lane & 3;
                const u32x4 a = *(const u32x4*)(p.ssmk + (size_t)(4 * bid + (j & 3)) * S);
                WAIT_VM0();
                u32x4 w = a;
                if (j >> 2) w = u32x4{a[0] << 16, __builtin_amdgcn_alignbit(a[1], a[0], 16), __builtin_amdgcn_alignbit(a[2], a[1], 16), __builtin_amdgcn_alignbit(a[3], a[2], 16)};
                if (c < 2) w = u32x4{0u, 0u, 0u, 0u};
                if (lane < 32 && c < 3) *(LAS u32x4*)(lds + L_H + ((j & 3) * H_CH + (j >> 2) * H_CS) * 4 + c * 16) = w;
                lds_post(lds + M_CRDY, lane);
                lds_wait<40>(lds + M_FULL, 12u);
                stage_w(1, 65536); stage_w(4, 81920);
                wait_free(13); stage_w(7, 0);
                wait_free(14); stage_w(10, 16384);
            }
            if (hw == 2) {
                lds_wait<40>(lds + M_FULL, 13u);
                u32 t[4];
#pragma unroll
                for (int i = 0; i < 4; ++i)
                    asm volatile("global_load_dword %0, %1, %2" : "=v"(t[i]) : "v"((lane >> 3) * 2048 + (lane & 7) * 128), "s"((const char*)p.wo + (hg >> 2) * 1024 + (ix * 32 + i * 8) * 2048) : "memory");
                asm volatile("s_waitcnt vmcnt(0)" : "+v"(t[0]), "+v"(t[1]), "+v"(t[2]), "+v"(t[3])::"memory");
            }
            if (hw == 3) {
#pragma unroll
                for (int i = 0; i < 2; ++i)
                    *(LAS u32x4*)(lds + L_U + (lane >> 4) * U_CH + i * U_G + 2048 + (lane & 15) * 16) = u32x4{0u, 0u, 0u, 0u};
                lds_wait<40>(lds + M_FULL, 12u);
                wait_tags32<16>(p.ws + WS_UF + (size_t)(bid >> 2) * 256, tag, lane);
#pragma unroll
                for (int jj = 0; jj < 8; ++jj) {
                    const char* ug = p.ws + WS_UT + (size_t)(4 * bid + (jj & 3)) * 4096 + (jj >> 2) * 16;
                    const u32 ul = ldsb + L_U + (jj & 3) * U_CH + (jj >> 2) * U_G;
                    dma_dev(lane * 32, ug, ul); dma_dev(lane * 32, ug + 2048, ul + 1024);
                }
                lds_post(lds + M_CRDY, lane);
            }
        }
        asm volatile("" : "+v"(xt[0]), "+v"(xt[1]), "+v"(xt[2]), "+v"(xt[3]), "+v"(dsk));
        {
            const int ch = 4 * bid + hw;
            const int pos = 15 - m - 8 * g;
            const u32 dv = dsk << (16 * (pos & 1));
            const u32x4 dg = {pos >> 1 == 0 ? dv : 0u, pos >> 1 == 1 ? dv : 0u, pos >> 1 == 2 ? dv : 0u, pos >> 1 == 3 ? dv : 0u};
            const char* zg = p.ws + WS_ZT + (size_t)ch * 4096;
            const u32 zo = (u32)(256 * (g & 1) + 16 * m + 8 * (g >> 1)) * 2;
            u32x16 cf0, cf1, cf2, cf3;
            lds_wait<40>(lds + M_FREE + hw * 256, 13u);
            lds_wait<12>(lds + M_CRDY, 2u);
    asm volatile(
        "ds_read_b128 v[192:195], %[b] offset:0\n\t"
        "ds_read_b128 v[188:191], %[b] offset:256\n\t"
        "ds_read_b128 v[184:187], %[b] offset:512\n\t"
        "ds_read_b128 v[180:183], %[b] offset:768\n\t"
        "ds_read_b128 v[176:179], %[b] offset:1024\n\t"
        "ds_read_b128 v[172:175], %[b] offset:1280\n\t"
        "ds_read_b128 v[168:171], %[b] offset:1536\n\t"
        "ds_read_b128 v[164:167], %[b] offset:1792\n\t"
        "ds_read2_b32 v[132:133], %[a0] offset0:0 offset1:1\n\t"
        "ds_read2_b32 v[134:135], %[a0] offset0:2 offset1:3\n\t"
        "ds_read2_b32 v[136:137], %[a0] offset0:128 offset1:129\n\t"
        "ds_read2_b32 v[138:139], %[a0] offset0:130 offset1:131\n\t"
        "ds_read2_b32 v[140:141], %[a1] offset0:0 offset1:1\n\t"
        "ds_read2_b32 v[142:143], %[a1] offset0:2 offset1:3\n\t"
        "ds_read2_b32 v[144:145], %[a1] offset0:128 offset1:129\n\t"
        "ds_read2_b32 v[146:147], %[a1] offset0:130 offset1:131\n\t"
        "ds_read2_b32 v[148:149], %[a2] offset0:0 offset1:1\n\t"
        "ds_read2_b32 v[150:151], %[a2] offset0:2 offset1:3\n\t"
        "ds_read2_b32 v[152:153], %[a2] offset0:128 offset1:129\n\t"
        "ds_read2_b32 v[154:155], %[a2] offset0:130 offset1:131\n\t"
        "ds_read2_b32 v[156:157], %[a3] offset0:0 offset1:1\n\t"
        "ds_read2_b32 v[158:159], %[a3] offset0:2 offset1:3\n\t"
        "ds_read2_b32 v[160:161], %[a3] offset0:128 offset1:129\n\t"
        "ds_read2_b32 v[162:163], %[a3] offset0:130 offset1:131\n\t"
        "s_waitcnt lgkmcnt(0)\n\t"
        : "=&{v[132:147]}"(cf0), "=&{v[148:163]}"(cf1), "=&{v[164:179]}"(cf2), "=&{v[180:195]}"(cf3) : [a0] "v"(ap), [a1] "v"(ap + 1024), [a2] "v"(ap + 2048), [a3] "v"(ap + 3072), [b] "v"(bp) : "memory");
            if (hw == 1) { wait_free(15); stage_w(13, 32768); __builtin_amdgcn_s_sleep(8); }
            else { lds_wait<12>(lds + M_FREE + hw * 256, 15u); __builtin_amdgcn_s_sleep(12); }
            lds_wait<2>(lds + M_FREE + hw * 256, 17u);
    asm volatile(
        "s_nop 1\n\t"
        "v_mfma_f32_16x16x32_bf16 v[128:131], v[132:135], v[192:195], 0\n\t"
        "ds_read_b128 v[224:227], %[b] offset:32\n\t"
        "v_mfma_f32_16x16x32_bf16 v[124:127], v[132:135], v[188:191], 0\n\t"
        "ds_read_b128 v[220:223], %[b] offset:288\n\t"
        "v_mfma_f32_16x16x32_bf16 v[120:123], v[132:135], v[184:187], 0\n\t"
        "ds_read_b128 v[216:219], %[b] offset:544\n\t"
        "v_mfma_f32_16x16x32_bf16 v[116:119], v[132:135], v[180:183], 0\n\t"
        "ds_read_b128 v[212:215], %[b] offset:800\n\t"
        "v_mfma_f32_16x16x32_bf16 v[112:115], v[132:135], v[176:179], 0\n\t"
        "ds_read_b128 v[208:211], %[b] offset:1056\n\t"
        "v_mfma_f32_16x16x32_bf16 v[108:111], v[132:135], v[172:175], 0\n\t"
        "ds_read_b128 v[204:207], %[b] offset:1312\n\t"
        "v_mfma_f32_16x16x32_bf16 v[104:107], v[132:135], v[168:171], 0\n\t"
        "ds_read_b128 v[200:203], %[b] offset:1568\n\t"
        "v_mfma_f32_16x16x32_bf16 v[100:103], v[132:135], v[164:167], 0\n\t"
        "ds_read2_b32 v[132:133], %[a0] offset0:16 offset1:17\n\t"
        "v_mfma_f32_16x16x32_bf16 v[128:131], v[136:139], v[188:191], v[128:131]\n\t"
        "ds_read2_b32 v[134:135], %[a0] offset0:18 offset1:19\n\t"
        "v_mfma_f32_16x16x32_bf16 v[128:131], %[dg], v[192:195], v[128:131]\n\t"
        "v_mfma_f32_16x16x32_bf16 v[124:127], v[136:139], v[184:187], v[124:127]\n\t"
        "ds_read_b128 v[196:199], %[b] offset:1824\n\t"
        "v_mfma_f32_16x16x32_bf16 v[124:127], %[dg], v[188:191], v[124:127]\n\t"
        "v_mfma_f32_16x16x32_bf16 v[120:123], v[136:139], v[180:183], v[120:123]\n\t"
        "v_mfma_f32_16x16x32_bf16 v[120:123], %[dg], v[184:187], v[120:123]\n\t"
        "v_mfma_f32_16x16x32_bf16 v[116:119], v[136:139], v[176:179], v[116:119]\n\t"
        "v_mfma_f32_16x16x32_bf16 v[116:119], %[dg], v[180:183], v[116:119]\n\t"
        "v_mfma_f32_16x16x32_bf16 v[112:115], v[136:139], v[172:175], v[112:115]\n\t"
        "v_mfma_f32_16x16x32_bf16 v[112:115], %[dg], v[176:179], v[112:115]\n\t"
        "v_mfma_f32_16x16x32_bf16 v[108:111], v[136:139], v[168:171], v[108:111]\n\t"
        "v_mfma_f32_16x16x32_bf16 v[108:111], %[dg], v[172:175], v[108:111]\n\t"
        "v_mfma_f32_16x16x32_bf16 v[104:107], v[136:139], v[164:167], v[104:107]\n\t"
        "ds_read2_b32 v[136:137], %[a0] offset0:144 offset1:145\n\t"
        "v_mfma_f32_16x16x32_bf16 v[104:107], %[dg], v[168:171], v[104:107]\n\t"
        "v_mfma_f32_16x16x32_bf16 v[100:103], %[dg], v[164:167], v[100:103]\n\t"
        "v_mfma_f32_16x16x32_bf16 v[128:131], v[140:143], v[184:187], v[128:131]\n\t"
        "ds_read2_b32 v[138:139], %[a0] offset0:146 offset1:147\n\t"
        "v_mfma_f32_16x16x32_bf16 v[124:127], v[140:143], v[180:183], v[124:127]\n\t"
        "v_mfma_f32_16x16x32_bf16 v[120:123], v[140:143], v[176:179], v[120:123]\n\t"
        "v_mfma_f32_16x16x32_bf16 v[116:119], v[140:143], v[172:175], v[116:119]\n\t"
        "v_mfma_f32_16x16x32_bf16 v[112:115], v[140:143], v[168:171], v[112:115]\n\t"
        "v_mfma_f32_16x16x32_bf16 v[108:111], v[140:143], v[164:167], v[108:111]\n\t"
        "ds_read2_b32 v[140:141], %[a1] offset0:16 offset1:17\n\t"
        "v_mfma_f32_16x16x32_bf16 v[128:131], v[144:147], v[180:183], v[128:131]\n\t"
        "ds_read2_b32 v[142:143], %[a1] offset0:18 offset1:19\n\t"
        "v_mfma_f32_16x16x32_bf16 v[124:127], v[144:147], v[176:179], v[124:127]\n\t"
        "v_mfma_f32_16x16x32_bf16 v[120:123], v[144:147], v[172:175], v[120:123]\n\t"
        "v_mfma_f32_16x16x32_bf16 v[116:119], v[144:147], v[168:171], v[116:119]\n\t"
        "s_waitcnt lgkmcnt(6)\n\t"
        "v_mfma_f32_16x16x32_bf16 v[112:115], v[144:147], v[164:167], v[112:115]\n\t"
        "ds_read2_b32 v[144:145], %[a1] offset0:144 offset1:145\n\t"
        "v_mfma_f32_16x16x32_bf16 v[128:131], v[148:151], v[176:179], v[128:131]\n\t"
        "ds_read2_b32 v[146:147], %[a1] offset0:146 offset1:147\n\t"
        "v_mfma_f32_16x16x32_bf16 v[124:127], v[148:151], v[172:175], v[124:127]\n\t"
        "v_mfma_f32_16x16x32_bf16 v[120:123], v[148:151], v[168:171], v[120:123]\n\t"
        "v_mfma_f32_16x16x32_bf16 v[116:119], v[148:151], v[164:167], v[116:119]\n\t"
        "ds_read2_b32 v[148:149], %[a2] offset0:16 offset1:17\n\t"
        "v_mfma_f32_16x16x32_bf16 v[128:131], v[152:155], v[172:175], v[128:131]\n\t"
        "ds_read2_b32 v[150:151], %[a2] offset0:18 offset1:19\n\t"
        "v_mfma_f32_16x16x32_bf16 v[124:127], v[152:155], v[168:171], v[124:127]\n\t"
        "v_mfma_f32_16x16x32_bf16 v[120:123], v[152:155], v[164:167], v[120:123]\n\t"
        "ds_read2_b32 v[152:153], %[a2] offset0:144 offset1:145\n\t"
        "v_mfma_f32_16x16x32_bf16 v[128:131], v[156:159], v[168:171], v[128:131]\n\t"
        "ds_read2_b32 v[154:155], %[a2] offset0:146 offset1:147\n\t"
        "v_mfma_f32_16x16x32_bf16 v[124:127], v[156:159], v[164:167], v[124:127]\n\t"
        "ds_read2_b32 v[156:157], %[a3] offset0:16 offset1:17\n\t"
        "v_mfma_f32_16x16x32_bf16 v[128:131], v[160:163], v[164:167], v[128:131]\n\t"
        "s_waitcnt lgkmcnt(4)\n\t"
        "ds_read2_b32 v[158:159], %[a3] offset0:18 offset1:19\n\t"
        "v_mfma_f32_16x16x32_bf16 v[128:131], v[132:135], v[224:227], v[128:131]\n\t"
        "ds_read2_b32 v[160:161], %[a3] offset0:144 offset1:145\n\t"
        "v_mfma_f32_16x16x32_bf16 v[124:127], v[132:135], v[220:223], v[124:127]\n\t"
        "ds_read2_b32 v[162:163], %[a3] offset0:146 offset1:147\n\t"
        "v_mfma_f32_16x16x32_bf16 v[120:123], v[132:135], v[216:219], v[120:123]\n\t"
        "ds_read_b128 v[192:195], %[b] offset:64\n\t"
        "v_mfma_f32_16x16x32_bf16 v[116:119], v[132:135], v[212:215], v[116:119]\n\t"
        "ds_read_b128 v[188:191], %[b] offset:320\n\t"
        "v_mfma_f32_16x16x32_bf16 v[112:115], v[132:135], v[208:211], v[112:115]\n\t"
        "ds_read_b128 v[184:187], %[b] offset:576\n\t"
        "v_mfma_f32_16x16x32_bf16 v[108:111], v[132:135], v[204:207], v[108:111]\n\t"
        "ds_read_b128 v[180:183], %[b] offset:832\n\t"
        "v_mfma_f32_16x16x32_bf16 v[104:107], v[132:135], v[200:203], v[104:107]\n\t"
        "ds_read_b128 v[176:179], %[b] offset:1088\n\t"
        "v_mfma_f32_16x16x32_bf16 v[100:103], v[132:135], v[196:199], v[100:103]\n\t"
        "ds_read2_b32 v[132:133], %[a0] offset0:32 offset1:33\n\t"
        "v_mfma_f32_16x16x32_bf16 v[128:131], v[136:139], v[220:223], v[128:131]\n\t"
        "ds_read2_b32 v[134:135], %[a0] offset0:34 offset1:35\n\t"
        "v_mfma_f32_16x16x32_bf16 v[124:127], v[136:139], v[216:219], v[124:127]\n\t"
        "ds_read_b128 v[172:175], %[b] offset:1344\n\t"
        "v_mfma_f32_16x16x32_bf16 v[120:123], v[136:139], v[212:215], v[120:123]\n\t"
        "ds_read_b128 v[168:171], %[b] offset:1600\n\t"
        "v_mfma_f32_16x16x32_bf16 v[116:119], v[136:139], v[208:211], v[116:119]\n\t"
        "ds_read_b128 v[164:167], %[b] offset:1856\n\t"
        "v_mfma_f32_16x16x32_bf16 v[112:115], v[136:139], v[204:207], v[112:115]\n\t"
        "s_waitcnt vmcnt(0)\n\t"
        "ds_add_u32 %[pa], %[pv]\n\t"
        "v_mfma_f32_16x16x32_bf16 v[108:111], v[136:139], v[200:203], v[108:111]\n\t"
        "v_mfma_f32_16x16x32_bf16 v[104:107], v[136:139], v[196:199], v[104:107]\n\t"
        "ds_read2_b32 v[136:137], %[a0] offset0:160 offset1:161\n\t"
        "v_mfma_f32_16x16x32_bf16 v[128:131], v[140:143], v[216:219], v[128:131]\n\t"
        "ds_read2_b32 v[138:139], %[a0] offset0:162 offset1:163\n\t"
        "v_mfma_f32_16x16x32_bf16 v[124:127], v[140:143], v[212:215], v[124:127]\n\t"
        "v_mfma_f32_16x16x32_bf16 v[120:123], v[140:143], v[208:211], v[120:123]\n\t"
        "v_mfma_f32_16x16x32_bf16 v[116:119], v[140:143], v[204:207], v[116:119]\n\t"
        "v_mfma_f32_16x16x32_bf16 v[112:115], v[140:143], v[200:203], v[112:115]\n\t"
        "v_mfma_f32_16x16x32_bf16 v[108:111], v[140:143], v[196:199], v[108:111]\n\t"
        "ds_read2_b32 v[140:141], %[a1] offset0:32 offset1:33\n\t"
        "v_mfma_f32_16x16x32_bf16 v[128:131], v[144:147], v[212:215], v[128:131]\n\t"
        "ds_read2_b32 v[142:143], %[a1] offset0:34 offset1:35\n\t"
        "v_mfma_f32_16x16x32_bf16 v[124:127], v[144:147], v[208:211], v[124:127]\n\t"
        "v_mfma_f32_16x16x32_bf16 v[120:123], v[144:147], v[204:207], v[120:123]\n\t"
        "v_mfma_f32_16x16x32_bf16 v[116:119], v[144:147], v[200:203], v[116:119]\n\t"
        "s_waitcnt lgkmcnt(6)\n\t"
        "v_mfma_f32_16x16x32_bf16 v[112:115], v[144:147], v[196:199], v[112:115]\n\t"
        "ds_read2_b32 v[144:145], %[a1] offset0:160 offset1:161\n\t"
        "v_mfma_f32_16x16x32_bf16 v[128:131], v[148:151], v[208:211], v[128:131]\n\t"
        "ds_read2_b32 v[146:147], %[a1] offset0:162 offset1:163\n\t"
        "v_mfma_f32_16x16x32_bf16 v[124:127], v[148:151], v[204:207], v[124:127]\n\t"
        "v_mfma_f32_16x16x32_bf16 v[120:123], v[148:151], v[200:203], v[120:123]\n\t"
        "v_mfma_f32_16x16x32_bf16 v[116:119], v[148:151], v[196:199], v[116:119]\n\t"
        "ds_read2_b32 v[148:149], %[a2] offset0:32 offset1:33\n\t"
        "v_mfma_f32_16x16x32_bf16 v[128:131], v[152:155], v[204:207], v[128:131]\n\t"
        "ds_read2_b32 v[150:151], %[a2] offset0:34 offset1:35\n\t"
        "v_mfma_f32_16x16x32_bf16 v[124:127], v[152:155], v[200:203], v[124:127]\n\t"
        "v_mfma_f32_16x16x32_bf16 v[120:123], v[152:155], v[196:199], v[120:123]\n\t"
        "ds_read2_b32 v[152:153], %[a2] offset0:160 offset1:161\n\t"
        "v_mfma_f32_16x16x32_bf16 v[128:131], v[156:159], v[200:203], v[128:131]\n\t"
        "ds_read2_b32 v[154:155], %[a2] offset0:162 offset1:163\n\t"
        "v_mfma_f32_16x16x32_bf16 v[124:127], v[156:159], v[196:199], v[124:127]\n\t"
        "ds_read2_b32 v[156:157], %[a3] offset0:32 offset1:33\n\t"
        "v_mfma_f32_16x16x32_bf16 v[128:131], v[160:163], v[196:199], v[128:131]\n\t"
        "s_waitcnt lgkmcnt(4)\n\t"
        "ds_read2_b32 v[158:159], %[a3] offset0:34 offset1:35\n\t"
        "v_mfma_f32_16x16x32_bf16 v[128:131], v[132:135], v[192:195], v[128:131]\n\t"
        "ds_read2_b32 v[160:161], %[a3] offset0:160 offset1:161\n\t"
        "v_mfma_f32_16x16x32_bf16 v[124:127], v[132:135], v[188:191], v[124:127]\n\t"
        "ds_read2_b32 v[162:163], %[a3] offset0:162 offset1:163\n\t"
        "v_mfma_f32_16x16x32_bf16 v[120:123], v[132:135], v[184:187], v[120:123]\n\t"
        "ds_read_b128 v[224:227], %[b] offset:96\n\t"
        "v_mfma_f32_16x16x32_bf16 v[116:119], v[132:135], v[180:183], v[116:119]\n\t"
        "ds_read_b128 v[220:223], %[b] offset:352\n\t"
        "v_mfma_f32_16x16x32_bf16 v[112:115], v[132:135], v[176:179], v[112:115]\n\t"
        "ds_read_b128 v[216:219], %[b] offset:608\n\t"
        "v_mfma_f32_16x16x32_bf16 v[108:111], v[132:135], v[172:175], v[108:111]\n\t"
        "ds_read_b128 v[212:215], %[b] offset:864\n\t"
        "v_mfma_f32_16x16x32_bf16 v[104:107], v[132:135], v[168:171], v[104:107]\n\t"
        "ds_read_b128 v[208:211], %[b] offset:1120\n\t"
        "v_mfma_f32_16x16x32_bf16 v[100:103], v[132:135], v[164:167], v[100:103]\n\t"
        "ds_read2_b32 v[132:133], %[a0] offset0:48 offset1:49\n\t"
        "v_mfma_f32_16x16x32_bf16 v[128:131], v[136:139], v[188:191], v[128:131]\n\t"
        "ds_read2_b32 v[134:135], %[a0] offset0:50 offset1:51\n\t"
        "v_mfma_f32_16x16x32_bf16 v[124:127], v[136:139], v[184:187], v[124:127]\n\t"
        "ds_read_b128 v[204:207], %[b] offset:1376\n\t"
        "v_mfma_f32_16x16x32_bf16 v[120:123], v[136:139], v[180:183], v[120:123]\n\t"
        "ds_read_b128 v[200:203], %[b] offset:1632\n\t"
        "v_mfma_f32_16x16x32_bf16 v[116:119], v[136:139], v[176:179], v[116:119]\n\t"
        "ds_read_b128 v[196:199], %[b] offset:1888\n\t"
        "v_mfma_f32_16x16x32_bf16 v[112:115], v[136:139], v[172:175], v[112:115]\n\t"
        "v_mfma_f32_16x16x32_bf16 v[108:111], v[136:139], v[168:171], v[108:111]\n\t"
        "v_mfma_f32_16x16x32_bf16 v[104:107], v[136:139], v[164:167], v[104:107]\n\t"
        "ds_read2_b32 v[136:137], %[a0] offset0:176 offset1:177\n\t"
        "v_mfma_f32_16x16x32_bf16 v[128:131], v[140:143], v[184:187], v[128:131]\n\t"
        "ds_read2_b32 v[138:139], %[a0] offset0:178 offset1:179\n\t"
        "v_mfma_f32_16x16x32_bf16 v[124:127], v[140:143], v[180:183], v[124:127]\n\t"
        "v_mfma_f32_16x16x32_bf16 v[120:123], v[140:143], v[176:179], v[120:123]\n\t"
        "v_mfma_f32_16x16x32_bf16 v[116:119], v[140:143], v[172:175], v[116:119]\n\t"
        "v_mfma_f32_16x16x32_bf16 v[112:115], v[140:143], v[168:171], v[112:115]\n\t"
        "v_mfma_f32_16x16x32_bf16 v[108:111], v[140:143], v[164:167], v[108:111]\n\t"
        "ds_read2_b32 v[140:141], %[a1] offset0:48 offset1:49\n\t"
        "v_mfma_f32_16x16x32_bf16 v[128:131], v[144:147], v[180:183], v[128:131]\n\t"
        "ds_read2_b32 v[142:143], %[a1] offset0:50 offset1:51\n\t"
        "v_mfma_f32_16x16x32_bf16 v[124:127], v[144:147], v[176:179], v[124:127]\n\t"
        "v_mfma_f32_16x16x32_bf16 v[120:123], v[144:147], v[172:175], v[120:123]\n\t"
        "v_mfma_f32_16x16x32_bf16 v[116:119], v[144:147], v[168:171], v[116:119]\n\t"
        "s_waitcnt lgkmcnt(6)\n\t"
        "v_mfma_f32_16x16x32_bf16 v[112:115], v[144:147], v[164:167], v[112:115]\n\t"
        "ds_read2_b32 v[144:145], %[a1] offset0:176 offset1:177\n\t"
        "v_mfma_f32_16x16x32_bf16 v[128:131], v[148:151], v[176:179], v[128:131]\n\t"
        "ds_read2_b32 v[146:147], %[a1] offset0:178 offset1:179\n\t"
        "v_mfma_f32_16x16x32_bf16 v[124:127], v[148:151], v[172:175], v[124:127]\n\t"
        "v_mfma_f32_16x16x32_bf16 v[120:123], v[148:151], v[168:171], v[120:123]\n\t"
        "v_mfma_f32_16x16x32_bf16 v[116:119], v[148:151], v[164:167], v[116:119]\n\t"
        "ds_read2_b32 v[148:149], %[a2] offset0:48 offset1:49\n\t"
        "v_mfma_f32_16x16x32_bf16 v[128:131], v[152:155], v[172:175], v[128:131]\n\t"
        "ds_read2_b32 v[150:151], %[a2] offset0:50 offset1:51\n\t"
        "v_mfma_f32_16x16x32_bf16 v[124:127], v[152:155], v[168:171], v[124:127]\n\t"
        "v_mfma_f32_16x16x32_bf16 v[120:123], v[152:155], v[164:167], v[120:123]\n\t"
        "ds_read2_b32 v[152:153], %[a2] offset0:176 offset1:177\n\t"
        "v_mfma_f32_16x16x32_bf16 v[128:131], v[156:159], v[168:171], v[128:131]\n\t"
        "ds_read2_b32 v[154:155], %[a2] offset0:178 offset1:179\n\t"
        "v_mfma_f32_16x16x32_bf16 v[124:127], v[156:159], v[164:167], v[124:127]\n\t"
        "ds_read2_b32 v[156:157], %[a3] offset0:48 offset1:49\n\t"
        "v_mfma_f32_16x16x32_bf16 v[128:131], v[160:163], v[164:167], v[128:131]\n\t"
        "s_waitcnt lgkmcnt(4)\n\t"
        "ds_read2_b32 v[158:159], %[a3] offset0:50 offset1:51\n\t"
        "v_mfma_f32_16x16x32_bf16 v[128:131], v[132:135], v[224:227], v[128:131]\n\t"
        "ds_read2_b32 v[160:161], %[a3] offset0:176 offset1:177\n\t"
        "v_mfma_f32_16x16x32_bf16 v[124:127], v[132:135], v[220:223], v[124:127]\n\t"
        "ds_read2_b32 v[162:163], %[a3] offset0:178 offset1:179\n\t"
        "v_mfma_f32_16x16x32_bf16 v[120:123], v[132:135], v[216:219], v[120:123]\n\t"
        "ds_read_b128 v[192:195], %[b] offset:128\n\t"
        "v_mfma_f32_16x16x32_bf16 v[116:119], v[132:135], v[212:215], v[116:119]\n\t"
        "ds_read_b128 v[188:191], %[b] offset:384\n\t"
        "v_mfma_f32_16x16x32_bf16 v[112:115], v[132:135], v[208:211], v[112:115]\n\t"
        "ds_read_b128 v[184:187], %[b] offset:640\n\t"
        "v_mfma_f32_16x16x32_bf16 v[108:111], v[132:135], v[204:207], v[108:111]\n\t"
        "ds_read_b128 v[180:183], %[b] offset:896\n\t"
        "v_mfma_f32_16x16x32_bf16 v[104:107], v[132:135], v[200:203], v[104:107]\n\t"
        "ds_read_b128 v[176:179], %[b] offset:1152\n\t"
        "v_mfma_f32_16x16x32_bf16 v[100:103], v[132:135], v[196:199], v[100:103]\n\t"
        "ds_read2_b32 v[132:133], %[a0] offset0:64 offset1:65\n\t"
        "v_mfma_f32_16x16x32_bf16 v[128:131], v[136:139], v[220:223], v[128:131]\n\t"
        "ds_read2_b32 v[134:135], %[a0] offset0:66 offset1:67\n\t"
        "v_mfma_f32_16x16x32_bf16 v[124:127], v[136:139], v[216:219], v[124:127]\n\t"
        "ds_read_b128 v[172:175], %[b] offset:1408\n\t"
        "v_mfma_f32_16x16x32_bf16 v[120:123], v[136:139], v[212:215], v[120:123]\n\t"
        "ds_read_b128 v[168:171], %[b] offset:1664\n\t"
        "v_mfma_f32_16x16x32_bf16 v[116:119], v[136:139], v[208:211], v[116:119]\n\t"
        "ds_read_b128 v[164:167], %[b] offset:1920\n\t"
        "v_mfma_f32_16x16x32_bf16 v[112:115], v[136:139], v[204:207], v[112:115]\n\t"
        "v_mfma_f32_16x16x32_bf16 v[108:111], v[136:139], v[200:203], v[108:111]\n\t"
        "v_mfma_f32_16x16x32_bf16 v[104:107], v[136:139], v[196:199], v[104:107]\n\t"
        "ds_read2_b32 v[136:137], %[a0] offset0:192 offset1:193\n\t"
        "v_mfma_f32_16x16x32_bf16 v[128:131], v[140:143], v[216:219], v[128:131]\n\t"
        "ds_read2_b32 v[138:139], %[a0] offset0:194 offset1:195\n\t"
        "v_mfma_f32_16x16x32_bf16 v[124:127], v[140:143], v[212:215], v[124:127]\n\t"
        "v_mfma_f32_16x16x32_bf16 v[120:123], v[140:143], v[208:211], v[120:123]\n\t"
        "v_mfma_f32_16x16x32_bf16 v[116:119], v[140:143], v[204:207], v[116:119]\n\t"
        "v_mfma_f32_16x16x32_bf16 v[112:115], v[140:143], v[200:203], v[112:115]\n\t"
        "v_mfma_f32_16x16x32_bf16 v[108:111], v[140:143], v[196:199], v[108:111]\n\t"
        "ds_read2_b32 v[140:141], %[a1] offset0:64 offset1:65\n\t"
        "v_mfma_f32_16x16x32_bf16 v[128:131], v[144:147], v[212:215], v[128:131]\n\t"
        "ds_read2_b32 v[142:143], %[a1] offset0:66 offset1:67\n\t"
        "v_mfma_f32_16x16x32_bf16 v[124:127], v[144:147], v[208:211], v[124:127]\n\t"
        "v_mfma_f32_16x16x32_bf16 v[120:123], v[144:147], v[204:207], v[120:123]\n\t"
        "v_mfma_f32_16x16x32_bf16 v[116:119], v[144:147], v[200:203], v[116:119]\n\t"
        "s_waitcnt lgkmcnt(6)\n\t"
        "v_mfma_f32_16x16x32_bf16 v[112:115], v[144:147], v[196:199], v[112:115]\n\t"
        "ds_read2_b32 v[144:145], %[a1] offset0:192 offset1:193\n\t"
        "v_mfma_f32_16x16x32_bf16 v[128:131], v[148:151], v[208:211], v[128:131]\n\t"
        "ds_read2_b32 v[146:147], %[a1] offset0:194 offset1:195\n\t"
        "v_mfma_f32_16x16x32_bf16 v[124:127], v[148:151], v[204:207], v[124:127]\n\t"
        "v_mfma_f32_16x16x32_bf16 v[120:123], v[148:151], v[200:203], v[120:123]\n\t"
        "v_mfma_f32_16x16x32_bf16 v[116:119], v[148:151], v[196:199], v[116:119]\n\t"
        "ds_read2_b32 v[148:149], %[a2] offset0:64 offset1:65\n\t"
        "v_mfma_f32_16x16x32_bf16 v[128:131], v[152:155], v[204:207], v[128:131]\n\t"
        "ds_read2_b32 v[150:151], %[a2] offset0:66 offset1:67\n\t"
        "v_mfma_f32_16x16x32_bf16 v[124:127], v[152:155], v[200:203], v[124:127]\n\t"
        "v_mfma_f32_16x16x32_bf16 v[120:123], v[152:155], v[196:199], v[120:123]\n\t"
        "ds_read2_b32 v[152:153], %[a2] offset0:192 offset1:193\n\t"
        "v_mfma_f32_16x16x32_bf16 v[128:131], v[156:159], v[200:203], v[128:131]\n\t"
        "ds_read2_b32 v[154:155], %[a2] offset0:194 offset1:195\n\t"
        "v_mfma_f32_16x16x32_bf16 v[124:127], v[156:159], v[196:199], v[124:127]\n\t"
        "ds_read2_b32 v[156:157], %[a3] offset0:64 offset1:65\n\t"
        "v_mfma_f32_16x16x32_bf16 v[128:131], v[160:163], v[196:199], v[128:131]\n\t"
        "s_waitcnt lgkmcnt(4)\n\t"
        "ds_read2_b32 v[158:159], %[a3] offset0:66 offset1:67\n\t"
        "v_mfma_f32_16x16x32_bf16 v[128:131], v[132:135], v[192:195], v[128:131]\n\t"
        "ds_read2_b32 v[160:161], %[a3] offset0:192 offset1:193\n\t"
        "v_mfma_f32_16x16x32_bf16 v[124:127], v[132:135], v[188:191], v[124:127]\n\t"
        "ds_read2_b32 v[162:163], %[a3] offset0:194 offset1:195\n\t"
        "v_mfma_f32_16x16x32_bf16 v[120:123], v[132:135], v[184:187], v[120:123]\n\t"
        "ds_read_b128 v[224:227], %[b] offset:160\n\t"
        "v_mfma_f32_16x16x32_bf16 v[116:119], v[132:135], v[180:183], v[116:119]\n\t"
        "ds_read_b128 v[220:223], %[b] offset:416\n\t"
        "v_mfma_f32_16x16x32_bf16 v[112:115], v[132:135], v[176:179], v[112:115]\n\t"
        "ds_read_b128 v[216:219], %[b] offset:672\n\t"
        "v_mfma_f32_16x16x32_bf16 v[108:111], v[132:135], v[172:175], v[108:111]\n\t"
        "ds_read_b128 v[212:215], %[b] offset:928\n\t"
        "v_mfma_f32_16x16x32_bf16 v[104:107], v[132:135], v[168:171], v[104:107]\n\t"
        "ds_read_b128 v[208:211], %[b] offset:1184\n\t"
        "v_mfma_f32_16x16x32_bf16 v[100:103], v[132:135], v[164:167], v[100:103]\n\t"
        "ds_read2_b32 v[132:133], %[a0] offset0:80 offset1:81\n\t"
        "v_mfma_f32_16x16x32_bf16 v[128:131], v[136:139], v[188:191], v[128:131]\n\t"
        "ds_read2_b32 v[134:135], %[a0] offset0:82 offset1:83\n\t"
        "v_mfma_f32_16x16x32_bf16 v[124:127], v[136:139], v[184:187], v[124:127]\n\t"
        "ds_read_b128 v[204:207], %[b] offset:1440\n\t"
        "v_mfma_f32_16x16x32_bf16 v[120:123], v[136:139], v[180:183], v[120:123]\n\t"
        "ds_read_b128 v[200:203], %[b] offset:1696\n\t"
        "v_mfma_f32_16x16x32_bf16 v[116:119], v[136:139], v[176:179], v[116:119]\n\t"
        "ds_read_b128 v[196:199], %[b] offset:1952\n\t"
        "v_mfma_f32_16x16x32_bf16 v[112:115], v[136:139], v[172:175], v[112:115]\n\t"
        "v_mfma_f32_16x16x32_bf16 v[108:111], v[136:139], v[168:171], v[108:111]\n\t"
        "v_mfma_f32_16x16x32_bf16 v[104:107], v[136:139], v[164:167], v[104:107]\n\t"
        "ds_read2_b32 v[136:137], %[a0] offset0:208 offset1:209\n\t"
        "v_mfma_f32_16x16x32_bf16 v[128:131], v[140:143], v[184:187], v[128:131]\n\t"
        "ds_read2_b32 v[138:139], %[a0] offset0:210 offset1:211\n\t"
        "v_mfma_f32_16x16x32_bf16 v[124:127], v[140:143], v[180:183], v[124:127]\n\t"
        "v_mfma_f32_16x16x32_bf16 v[120:123], v[140:143], v[176:179], v[120:123]\n\t"
        "v_mfma_f32_16x16x32_bf16 v[116:119], v[140:143], v[172:175], v[116:119]\n\t"
        "v_mfma_f32_16x16x32_bf16 v[112:115], v[140:143], v[168:171], v[112:115]\n\t"
        "v_mfma_f32_16x16x32_bf16 v[108:111], v[140:143], v[164:167], v[108:111]\n\t"
        "ds_read2_b32 v[140:141], %[a1] offset0:80 offset1:81\n\t"
        "v_mfma_f32_16x16x32_bf16 v[128:131], v[144:147], v[180:183], v[128:131]\n\t"
        "ds_read2_b32 v[142:143], %[a1] offset0:82 offset1:83\n\t"
        "v_mfma_f32_16x16x32_bf16 v[124:127], v[144:147], v[176:179], v[124:127]\n\t"
        "v_mfma_f32_16x16x32_bf16 v[120:123], v[144:147], v[172:175], v[120:123]\n\t"
        "v_mfma_f32_16x16x32_bf16 v[116:119], v[144:147], v[168:171], v[116:119]\n\t"
        "s_waitcnt lgkmcnt(6)\n\t"
        "v_mfma_f32_16x16x32_bf16 v[112:115], v[144:147], v[164:167], v[112:115]\n\t"
        "ds_read2_b32 v[144:145], %[a1] offset0:208 offset1:209\n\t"
        "v_mfma_f32_16x16x32_bf16 v[128:131], v[148:151], v[176:179], v[128:131]\n\t"
        "ds_read2_b32 v[146:147], %[a1] offset0:210 offset1:211\n\t"
        "v_mfma_f32_16x16x32_bf16 v[124:127], v[148:151], v[172:175], v[124:127]\n\t"
        "v_mfma_f32_16x16x32_bf16 v[120:123], v[148:151], v[168:171], v[120:123]\n\t"
        "v_mfma_f32_16x16x32_bf16 v[116:119], v[148:151], v[164:167], v[116:119]\n\t"
        "ds_read2_b32 v[148:149], %[a2] offset0:80 offset1:81\n\t"
        "v_mfma_f32_16x16x32_bf16 v[128:131], v[152:155], v[172:175], v[128:131]\n\t"
        "ds_read2_b32 v[150:151], %[a2] offset0:82 offset1:83\n\t"
        "v_mfma_f32_16x16x32_bf16 v[124:127], v[152:155], v[168:171], v[124:127]\n\t"
        "v_mfma_f32_16x16x32_bf16 v[120:123], v[152:155], v[164:167], v[120:123]\n\t"
        "ds_read2_b32 v[152:153], %[a2] offset0:208 offset1:209\n\t"
        "v_mfma_f32_16x16x32_bf16 v[128:131], v[156:159], v[168:171], v[128:131]\n\t"
        "ds_read2_b32 v[154:155], %[a2] offset0:210 offset1:211\n\t"
        "v_mfma_f32_16x16x32_bf16 v[124:127], v[156:159], v[164:167], v[124:127]\n\t"
        "ds_read2_b32 v[156:157], %[a3] offset0:80 offset1:81\n\t"
        "v_mfma_f32_16x16x32_bf16 v[128:131], v[160:163], v[164:167], v[128:131]\n\t"
        "s_waitcnt lgkmcnt(4)\n\t"
        "ds_read2_b32 v[158:159], %[a3] offset0:82 offset1:83\n\t"
        "v_mfma_f32_16x16x32_bf16 v[128:131], v[132:135], v[224:227], v[128:131]\n\t"
        "ds_read2_b32 v[160:161], %[a3] offset0:208 offset1:209\n\t"
        "v_mfma_f32_16x16x32_bf16 v[124:127], v[132:135], v[220:223], v[124:127]\n\t"
        "ds_read2_b32 v[162:163], %[a3] offset0:210 offset1:211\n\t"
        "v_mfma_f32_16x16x32_bf16 v[120:123], v[132:135], v[216:219], v[120:123]\n\t"
        "ds_read_b128 v[192:195], %[b] offset:192\n\t"
        "v_mfma_f32_16x16x32_bf16 v[116:119], v[132:135], v[212:215], v[116:119]\n\t"
        "ds_read_b128 v[188:191], %[b] offset:448\n\t"
        "v_mfma_f32_16x16x32_bf16 v[112:115], v[132:135], v[208:211], v[112:115]\n\t"
        "ds_read_b128 v[184:187], %[b] offset:704\n\t"
        "v_mfma_f32_16x16x32_bf16 v[108:111], v[132:135], v[204:207], v[108:111]\n\t"
        "ds_read_b128 v[180:183], %[b] offset:960\n\t"
        "v_mfma_f32_16x16x32_bf16 v[104:107], v[132:135], v[200:203], v[104:107]\n\t"
        "ds_read_b128 v[176:179], %[b] offset:1216\n\t"
        "v_mfma_f32_16x16x32_bf16 v[100:103], v[132:135], v[196:199], v[100:103]\n\t"
        "ds_read2_b32 v[132:133], %[a0] offset0:96 offset1:97\n\t"
        "v_mfma_f32_16x16x32_bf16 v[128:131], v[136:139], v[220:223], v[128:131]\n\t"
        "ds_read2_b32 v[134:135], %[a0] offset0:98 offset1:99\n\t"
        "v_mfma_f32_16x16x32_bf16 v[124:127], v[136:139], v[216:219], v[124:127]\n\t"
        "ds_read_b128 v[172:175], %[b] offset:1472\n\t"
        "v_mfma_f32_16x16x32_bf16 v[120:123], v[136:139], v[212:215], v[120:123]\n\t"
        "ds_read_b128 v[168:171], %[b] offset:1728\n\t"
        "v_mfma_f32_16x16x32_bf16 v[116:119], v[136:139], v[208:211], v[116:119]\n\t"
        "ds_read_b128 v[164:167], %[b] offset:1984\n\t"
        "v_mfma_f32_16x16x32_bf16 v[112:115], v[136:139], v[204:207], v[112:115]\n\t"
        "v_mfma_f32_16x16x32_bf16 v[108:111], v[136:139], v[200:203], v[108:111]\n\t"
        "v_mfma_f32_16x16x32_bf16 v[104:107], v[136:139], v[196:199], v[104:107]\n\t"
        "ds_read2_b32 v[136:137], %[a0] offset0:224 offset1:225\n\t"
        "v_mfma_f32_16x16x32_bf16 v[128:131], v[140:143], v[216:219], v[128:131]\n\t"
        "ds_read2_b32 v[138:139], %[a0] offset0:226 offset1:227\n\t"
        "v_mfma_f32_16x16x32_bf16 v[124:127], v[140:143], v[212:215], v[124:127]\n\t"
        "v_mfma_f32_16x16x32_bf16 v[120:123], v[140:143], v[208:211], v[120:123]\n\t"
        "v_mfma_f32_16x16x32_bf16 v[116:119], v[140:143], v[204:207], v[116:119]\n\t"
        "v_mfma_f32_16x16x32_bf16 v[112:115], v[140:143], v[200:203], v[112:115]\n\t"
        "v_mfma_f32_16x16x32_bf16 v[108:111], v[140:143], v[196:199], v[108:111]\n\t"
        "ds_read2_b32 v[140:141], %[a1] offset0:96 offset1:97\n\t"
        "v_mfma_f32_16x16x32_bf16 v[128:131], v[144:147], v[212:215], v[128:131]\n\t"
        "ds_read2_b32 v[142:143], %[a1] offset0:98 offset1:99\n\t"
        "v_mfma_f32_16x16x32_bf16 v[124:127], v[144:147], v[208:211], v[124:127]\n\t"
        "v_mfma_f32_16x16x32_bf16 v[120:123], v[144:147], v[204:207], v[120:123]\n\t"
        "v_mfma_f32_16x16x32_bf16 v[116:119], v[144:147], v[200:203], v[116:119]\n\t"
        "s_waitcnt lgkmcnt(6)\n\t"
        "v_mfma_f32_16x16x32_bf16 v[112:115], v[144:147], v[196:199], v[112:115]\n\t"
        "ds_read2_b32 v[144:145], %[a1] offset0:224 offset1:225\n\t"
        "v_mfma_f32_16x16x32_bf16 v[128:131], v[148:151], v[208:211], v[128:131]\n\t"
        "ds_read2_b32 v[146:147], %[a1] offset0:226 offset1:227\n\t"
        "v_mfma_f32_16x16x32_bf16 v[124:127], v[148:151], v[204:207], v[124:127]\n\t"
        "v_mfma_f32_16x16x32_bf16 v[120:123], v[148:151], v[200:203], v[120:123]\n\t"
        "v_mfma_f32_16x16x32_bf16 v[116:119], v[148:151], v[196:199], v[116:119]\n\t"
        "ds_read2_b32 v[148:149], %[a2] offset0:96 offset1:97\n\t"
        "v_mfma_f32_16x16x32_bf16 v[128:131], v[152:155], v[204:207], v[128:131]\n\t"
        "ds_read2_b32 v[150:151], %[a2] offset0:98 offset1:99\n\t"
        "v_mfma_f32_16x16x32_bf16 v[124:127], v[152:155], v[200:203], v[124:127]\n\t"
        "v_mfma_f32_16x16x32_bf16 v[120:123], v[152:155], v[196:199], v[120:123]\n\t"
        "ds_read2_b32 v[152:153], %[a2] offset0:224 offset1:225\n\t"
        "v_mfma_f32_16x16x32_bf16 v[128:131], v[156:159], v[200:203], v[128:131]\n\t"
        "ds_read2_b32 v[154:155], %[a2] offset0:226 offset1:227\n\t"
        "v_mfma_f32_16x16x32_bf16 v[124:127], v[156:159], v[196:199], v[124:127]\n\t"
        "ds_read2_b32 v[156:157], %[a3] offset0:96 offset1:97\n\t"
        "v_mfma_f32_16x16x32_bf16 v[128:131], v[160:163], v[196:199], v[128:131]\n\t"
        "s_waitcnt lgkmcnt(4)\n\t"
        "ds_read2_b32 v[158:159], %[a3] offset0:98 offset1:99\n\t"
        "v_mfma_f32_16x16x32_bf16 v[128:131], v[132:135], v[192:195], v[128:131]\n\t"
        "ds_read2_b32 v[160:161], %[a3] offset0:224 offset1:225\n\t"
        "v_mfma_f32_16x16x32_bf16 v[124:127], v[132:135], v[188:191], v[124:127]\n\t"
        "ds_read2_b32 v[162:163], %[a3] offset0:226 offset1:227\n\t"
        "v_mfma_f32_16x16x32_bf16 v[120:123], v[132:135], v[184:187], v[120:123]\n\t"
        "ds_read_b128 v[224:227], %[b] offset:224\n\t"
        "v_mfma_f32_16x16x32_bf16 v[116:119], v[132:135], v[180:183], v[116:119]\n\t"
        "ds_read_b128 v[220:223], %[b] offset:480\n\t"
        "v_mfma_f32_16x16x32_bf16 v[112:115], v[132:135], v[176:179], v[112:115]\n\t"
        "ds_read_b128 v[216:219], %[b] offset:736\n\t"
        "v_mfma_f32_16x16x32_bf16 v[108:111], v[132:135], v[172:175], v[108:111]\n\t"
        "ds_read_b128 v[212:215], %[b] offset:992\n\t"
        "v_mfma_f32_16x16x32_bf16 v[104:107], v[132:135], v[168:171], v[104:107]\n\t"
        "ds_read_b128 v[208:211], %[b] offset:1248\n\t"
        "v_mfma_f32_16x16x32_bf16 v[100:103], v[132:135], v[164:167], v[100:103]\n\t"
        "ds_read2_b32 v[132:133], %[a0] offset0:112 offset1:113\n\t"
        "v_mfma_f32_16x16x32_bf16 v[128:131], v[136:139], v[188:191], v[128:131]\n\t"
        "ds_read2_b32 v[134:135], %[a0] offset0:114 offset1:115\n\t"
        "v_mfma_f32_16x16x32_bf16 v[124:127], v[136:139], v[184:187], v[124:127]\n\t"
        "ds_read_b128 v[204:207], %[b] offset:1504\n\t"
        "v_mfma_f32_16x16x32_bf16 v[120:123], v[136:139], v[180:183], v[120:123]\n\t"
        "ds_read_b128 v[200:203], %[b] offset:1760\n\t"
        "v_mfma_f32_16x16x32_bf16 v[116:119], v[136:139], v[176:179], v[116:119]\n\t"
        "ds_read_b128 v[196:199], %[b] offset:2016\n\t"
        "v_mfma_f32_16x16x32_bf16 v[112:115], v[136:139], v[172:175], v[112:115]\n\t"
        "v_mfma_f32_16x16x32_bf16 v[108:111], v[136:139], v[168:171], v[108:111]\n\t"
        "v_mfma_f32_16x16x32_bf16 v[104:107], v[136:139], v[164:167], v[104:107]\n\t"
        "ds_read2_b32 v[136:137], %[a0] offset0:240 offset1:241\n\t"
        "v_mfma_f32_16x16x32_bf16 v[128:131], v[140:143], v[184:187], v[128:131]\n\t"
        "ds_read2_b32 v[138:139], %[a0] offset0:242 offset1:243\n\t"
        "v_mfma_f32_16x16x32_bf16 v[124:127], v[140:143], v[180:183], v[124:127]\n\t"
        "v_mfma_f32_16x16x32_bf16 v[120:123], v[140:143], v[176:179], v[120:123]\n\t"
        "v_mfma_f32_16x16x32_bf16 v[116:119], v[140:143], v[172:175], v[116:119]\n\t"
        "v_mfma_f32_16x16x32_bf16 v[112:115], v[140:143], v[168:171], v[112:115]\n\t"
        "v_mfma_f32_16x16x32_bf16 v[108:111], v[140:143], v[164:167], v[108:111]\n\t"
        "ds_read2_b32 v[140:141], %[a1] offset0:112 offset1:113\n\t"
        "v_mfma_f32_16x16x32_bf16 v[128:131], v[144:147], v[180:183], v[128:131]\n\t"
        "ds_read2_b32 v[142:143], %[a1] offset0:114 offset1:115\n\t"
        "v_mfma_f32_16x16x32_bf16 v[124:127], v[144:147], v[176:179], v[124:127]\n\t"
        "v_mfma_f32_16x16x32_bf16 v[120:123], v[144:147], v[172:175], v[120:123]\n\t"
        "v_mfma_f32_16x16x32_bf16 v[116:119], v[144:147], v[168:171], v[116:119]\n\t"
        "s_waitcnt lgkmcnt(6)\n\t"
        "v_mfma_f32_16x16x32_bf16 v[112:115], v[144:147], v[164:167], v[112:115]\n\t"
        "ds_read2_b32 v[144:145], %[a1] offset0:240 offset1:241\n\t"
        "v_mfma_f32_16x16x32_bf16 v[128:131], v[148:151], v[176:179], v[128:131]\n\t"
        "ds_read2_b32 v[146:147], %[a1] offset0:242 offset1:243\n\t"
        "v_mfma_f32_16x16x32_bf16 v[124:127], v[148:151], v[172:175], v[124:127]\n\t"
        "v_mfma_f32_16x16x32_bf16 v[120:123], v[148:151], v[168:171], v[120:123]\n\t"
        "v_mfma_f32_16x16x32_bf16 v[116:119], v[148:151], v[164:167], v[116:119]\n\t"
        "ds_read2_b32 v[148:149], %[a2] offset0:112 offset1:113\n\t"
        "v_mfma_f32_16x16x32_bf16 v[128:131], v[152:155], v[172:175], v[128:131]\n\t"
        "ds_read2_b32 v[150:151], %[a2] offset0:114 offset1:115\n\t"
        "v_mfma_f32_16x16x32_bf16 v[124:127], v[152:155], v[168:171], v[124:127]\n\t"
        "v_mfma_f32_16x16x32_bf16 v[120:123], v[152:155], v[164:167], v[120:123]\n\t"
        "ds_read2_b32 v[152:153], %[a2] offset0:240 offset1:241\n\t"
        "v_mfma_f32_16x16x32_bf16 v[128:131], v[156:159], v[168:171], v[128:131]\n\t"
        "ds_read2_b32 v[154:155], %[a2] offset0:242 offset1:243\n\t"
        "v_mfma_f32_16x16x32_bf16 v[124:127], v[156:159], v[164:167], v[124:127]\n\t"
        "ds_read2_b32 v[156:157], %[a3] offset0:112 offset1:113\n\t"
        "v_mfma_f32_16x16x32_bf16 v[128:131], v[160:163], v[164:167], v[128:131]\n\t"
        "s_waitcnt lgkmcnt(4)\n\t"
        "ds_read2_b32 v[158:159], %[a3] offset0:114 offset1:115\n\t"
        "v_mfma_f32_16x16x32_bf16 v[128:131], v[132:135], v[224:227], v[128:131]\n\t"
        "ds_read2_b32 v[160:161], %[a3] offset0:240 offset1:241\n\t"
        "v_mfma_f32_16x16x32_bf16 v[124:127], v[132:135], v[220:223], v[124:127]\n\t"
        "ds_read2_b32 v[162:163], %[a3] offset0:242 offset1:243\n\t"
        "v_mfma_f32_16x16x32_bf16 v[120:123], v[132:135], v[216:219], v[120:123]\n\t"
        "v_mfma_f32_16x16x32_bf16 v[116:119], v[132:135], v[212:215], v[116:119]\n\t"
        "v_mfma_f32_16x16x32_bf16 v[112:115], v[132:135], v[208:211], v[112:115]\n\t"
        "v_mfma_f32_16x16x32_bf16 v[108:111], v[132:135], v[204:207], v[108:111]\n\t"
        "v_mfma_f32_16x16x32_bf16 v[104:107], v[132:135], v[200:203], v[104:107]\n\t"
        "v_mfma_f32_16x16x32_bf16 v[100:103], v[132:135], v[196:199], v[100:103]\n\t"
        "v_mfma_f32_16x16x32_bf16 v[128:131], v[136:139], v[220:223], v[128:131]\n\t"
        "v_mfma_f32_16x16x32_bf16 v[124:127], v[136:139], v[216:219], v[124:127]\n\t"
        "v_mfma_f32_16x16x32_bf16 v[120:123], v[136:139], v[212:215], v[120:123]\n\t"
        "v_mfma_f32_16x16x32_bf16 v[116:119], v[136:139], v[208:211], v[116:119]\n\t"
        "v_mfma_f32_16x16x32_bf16 v[112:115], v[136:139], v[204:207], v[112:115]\n\t"
        "v_mfma_f32_16x16x32_bf16 v[108:111], v[136:139], v[200:203], v[108:111]\n\t"
        "v_mfma_f32_16x16x32_bf16 v[104:107], v[136:139], v[196:199], v[104:107]\n\t"
        "v_mfma_f32_16x16x32_bf16 v[128:131], v[140:143], v[216:219], v[128:131]\n\t"
        "v_mfma_f32_16x16x32_bf16 v[124:127], v[140:143], v[212:215], v[124:127]\n\t"
        "v_mfma_f32_16x16x32_bf16 v[120:123], v[140:143], v[208:211], v[120:123]\n\t"
        "v_mfma_f32_16x16x32_bf16 v[116:119], v[140:143], v[204:207], v[116:119]\n\t"
        "v_mfma_f32_16x16x32_bf16 v[112:115], v[140:143], v[200:203], v[112:115]\n\t"
        "v_mfma_f32_16x16x32_bf16 v[108:111], v[140:143], v[196:199], v[108:111]\n\t"
        "v_mfma_f32_16x16x32_bf16 v[128:131], v[144:147], v[212:215], v[128:131]\n\t"
        "v_mfma_f32_16x16x32_bf16 v[124:127], v[144:147], v[208:211], v[124:127]\n\t"
        "v_mfma_f32_16x16x32_bf16 v[120:123], v[144:147], v[204:207], v[120:123]\n\t"
        "v_cvt_pk_bf16_f32 v164, v100, v101\n\t"
        "v_cvt_pk_bf16_f32 v166, v104, v105\n\t"
        "v_mfma_f32_16x16x32_bf16 v[116:119], v[144:147], v[200:203], v[116:119]\n\t"
        "s_waitcnt lgkmcnt(0)\n\t"
        "v_cvt_pk_bf16_f32 v165, v102, v103\n\t"
        "v_cvt_pk_bf16_f32 v167, v106, v107\n\t"
        "v_mfma_f32_16x16x32_bf16 v[112:115], v[144:147], v[196:199], v[112:115]\n\t"
        "v_permlane16_swap_b32 v164, v166\n\t"
        "v_mfma_f32_16x16x32_bf16 v[128:131], v[148:151], v[208:211], v[128:131]\n\t"
        "v_permlane16_swap_b32 v165, v167\n\t"
        "v_mfma_f32_16x16x32_bf16 v[124:127], v[148:151], v[204:207], v[124:127]\n\t"
        "global_store_dwordx4 %[zo], v[164:167], %[zg] offset:0 sc1\n\t"
        "v_mfma_f32_16x16x32_bf16 v[120:123], v[148:151], v[200:203], v[120:123]\n\t"
        "v_mfma_f32_16x16x32_bf16 v[116:119], v[148:151], v[196:199], v[116:119]\n\t"
        "v_mfma_f32_16x16x32_bf16 v[128:131], v[152:155], v[204:207], v[128:131]\n\t"
        "v_cvt_pk_bf16_f32 v168, v108, v109\n\t"
        "v_cvt_pk_bf16_f32 v170, v112, v113\n\t"
        "v_mfma_f32_16x16x32_bf16 v[124:127], v[152:155], v[200:203], v[124:127]\n\t"
        "v_cvt_pk_bf16_f32 v169, v110, v111\n\t"
        "v_cvt_pk_bf16_f32 v171, v114, v115\n\t"
        "v_mfma_f32_16x16x32_bf16 v[120:123], v[152:155], v[196:199], v[120:123]\n\t"
        "v_permlane16_swap_b32 v168, v170\n\t"
        "v_mfma_f32_16x16x32_bf16 v[128:131], v[156:159], v[200:203], v[128:131]\n\t"
        "v_permlane16_swap_b32 v169, v171\n\t"
        "v_mfma_f32_16x16x32_bf16 v[124:127], v[156:159], v[196:199], v[124:127]\n\t"
        "global_store_dwordx4 %[zo], v[168:171], %[zg] offset:1024 sc1\n\t"
        "v_mfma_f32_16x16x32_bf16 v[128:131], v[160:163], v[196:199], v[128:131]\n\t"
        "s_nop 3\n\t"
        "v_cvt_pk_bf16_f32 v172, v116, v117\n\t"
        "v_cvt_pk_bf16_f32 v174, v120, v121\n\t"
        "v_cvt_pk_bf16_f32 v173, v118, v119\n\t"
        "v_cvt_pk_bf16_f32 v175, v122, v123\n\t"
        "s_nop 1\n\t"
        "v_cvt_pk_bf16_f32 v176, v124, v125\n\t"
        "v_cvt_pk_bf16_f32 v178, v128, v129\n\t"
        "v_permlane16_swap_b32 v172, v174\n\t"
        "v_permlane16_swap_b32 v173, v175\n\t"
        "v_cvt_pk_bf16_f32 v177, v126, v127\n\t"
        "v_cvt_pk_bf16_f32 v179, v130, v131\n\t"
        "global_store_dwordx4 %[zo], v[172:175], %[zg] offset:2048 sc1\n\t"
        "s_nop 0\n\t"
        "v_permlane16_swap_b32 v176, v178\n\t"
        "v_permlane16_swap_b32 v177, v179\n\t"
        "global_store_dwordx4 %[zo], v[176:179], %[zg] offset:3072 sc1\n\t"
        "s_nop 1\n\t"
        : "+{v[132:147]}"(cf0), "+{v[148:163]}"(cf1), "+{v[164:179]}"(cf2), "+{v[180:195]}"(cf3)
        : [a0] "v"(ap), [a1] "v"(ap + 1024), [a2] "v"(ap + 2048), [a3] "v"(ap + 3072), [b] "v"(bp), [dg] "v"(dg), [pa] "v"(ldsb + M_WRDY + lane * 4), [pv] "v"(hw == 1 ? 1u : 0u), [zo] "v"(zo), [zg] "s"(zg)
        : "memory", "v100", "v101", "v102", "v103", "v104", "v105", "v106", "v107", "v108", "v109", "v110", "v111", "v112", "v113", "v114", "v115", "v116", "v117", "v118", "v119", "v120", "v121", "v122", "v123", "v124", "v125", "v126", "v127", "v128", "v129", "v130", "v131", "v196", "v197", "v198", "v199", "v200", "v201", "v202", "v203", "v204", "v205", "v206", "v207", "v208", "v209", "v210", "v211", "v212", "v213", "v214", "v215", "v216", "v217", "v218", "v219", "v220", "v221", "v222", "v223", "v224", "v225", "v226", "v227");
            auto scale_x = [&](int i) {
                const float gr = *(const LAS float*)(lds + M_G + (4 * (4 * hw + i) + (lane >> 4)) * 4);
#pragma unroll
                for (int e = 0; e < 4; ++e) xt[i][e] = pk2(mul1(gr, bflo(xt[i][e])), mul1(gr, bfhi(xt[i][e])));
            };
            auto issue_o = [&](int j, u32 slot, int i) {
                dma_dev(vl16, p.ws + WS_O + (size_t)rt * 131072 + ((rot3 + j) & 15) * 8192 + hw * 1024 + i * 4096, ldsb + slot + i * 4096 + hw * 1024);
            };
            auto issue_w = [&](int j, u32 slot, int i) {
                dma(voP, (const char*)p.wo + hg * 256 + hw * 64 + (size_t)(((rot3 + j) & 15) * 64 + i * 16) * 2048, ldsb + slot + hw * 4096 + i * 1024);
            };
            auto put_x = [&]() {
#pragma unroll
                for (int i = 0; i < 4; ++i) {
                    const int row = 4 * (4 * hw + i) + (lane >> 4);
                    *(LAS u32x4*)(lds + L_X + row * 256 + (((lane & 15) ^ (row & 15)) * 16)) = xt[i];
                }
            };
            u32x2 zf;
            auto zseen = [&]() { return __all(zf[0] == (u32)tag && zf[1] == (u32)(tag >> 32)); };
            bool zpend = true;
            auto zjoin = [&]() {
                u32 cnt;
                asm volatile("s_mov_b64 exec, 1\n\tds_add_rtn_u32 %0, %1, %2\n\ts_mov_b64 exec, -1\n\ts_waitcnt lgkmcnt(0)" : "=&v"(cnt) : "v"(ldsb + M_ZJ), "v"(1u) : "memory");
                if (rfl(cnt) == 3u && lane == 0) st8g_dev(p.ws + WS_ZF + bid * 8, tag);
                zpend = false;
            };
            auto zearly_publish = [&]() {
#pragma unroll 1
                for (u32 spins = 0; spins < spin_cap(0); ++spins) {
                    if (rfl(*(volatile LAS u32*)(lds + M_CR)) >= 4u) break;
                    if (vm_idle()) { WAIT_VM0(); zjoin(); break; }
                }
            };
            auto issue_z = [&]() {
#pragma unroll
                for (int i = 0; i < 4; ++i) {
                    const int row = 8 * (4 * hw + i) + (lane >> 3);
                    dma_dev(row * 4096 + (((lane & 7) ^ (((row >> 1) & 3) << 1)) * 16), p.ws + WS_ZT + (size_t)hg * 128 * 4096 + rt * 128, ldsb + L_Z + (4 * hw + i) * 1024);
                }
            };
            zearly_publish();
            BAR();
            issue_o(4, 108032, 0);
            issue_o(4, 108032, 1);
            issue_o(5, 116224, 0);
            issue_o(5, 116224, 1);
            BAR();
            issue_o(6, 124416, 0);
            issue_o(6, 124416, 1);
            issue_w(6, 65536, 0);
            issue_w(6, 65536, 1);
            BAR();
            issue_w(6, 65536, 2);
            issue_w(6, 65536, 3);
            issue_o(7, 132608, 0);
            issue_o(7, 132608, 1);
            BAR();
            issue_o(8, 32768, 0);
            issue_o(8, 32768, 1);
            issue_w(8, 81920, 0);
            issue_w(8, 81920, 1);
            WAIT_BAR(14);
            if (zpend) zjoin();
            issue_w(8, 81920, 2);
            issue_w(8, 81920, 3);
            issue_o(9, 40960, 0);
            issue_o(9, 40960, 1);
            WAIT_BAR(16);
            issue_w(9, 0, 0);
            issue_w(9, 0, 1);
            issue_w(9, 0, 2);
            issue_w(9, 0, 3);
            WAIT_BAR(14);
            issue_o(10, 49152, 0);
            issue_o(10, 49152, 1);
            issue_o(11, 57344, 0);
            issue_o(11, 57344, 1);
            WAIT_BAR(16);
            issue_w(11, 16384, 0);
            issue_w(11, 16384, 1);
            issue_w(11, 16384, 2);
            issue_w(11, 16384, 3);
            WAIT_BAR(14);
            issue_o(12, 108032, 0);
            issue_o(12, 108032, 1);
            issue_w(12, 65536, 0);
            issue_w(12, 65536, 1);
            WAIT_BAR(12);
            issue_w(12, 65536, 2);
            issue_w(12, 65536, 3);
            issue_o(13, 116224, 0);
            issue_o(13, 116224, 1);
            WAIT_BAR(14);
            issue_o(14, 124416, 0);
            issue_o(14, 124416, 1);
            issue_w(14, 81920, 0);
            issue_w(14, 81920, 1);
            if (hw == 3) ld8_issue(zf, (lane & 31) * 8, p.ws + WS_ZF + hg * 256);
            WAIT_BAR(12);
            issue_w(14, 81920, 2);
            issue_w(14, 81920, 3);
            issue_o(15, 132608, 0);
            issue_o(15, 132608, 1);
            WAIT_BAR(10);
            if (hw == 3) { asm volatile("s_waitcnt vmcnt(4)" : "+v"(zf)::"memory"); if (zseen()) *(volatile LAS u32*)(lds + M_ZOK) = 1u; }
            issue_w(15, 0, 0);
            issue_w(15, 0, 1);
            issue_w(15, 0, 2);
            issue_w(15, 0, 3);
            WAIT_BAR(12);
            const bool zearly = rfl(*(volatile LAS u32*)(lds + M_ZOK)) != 0;
            if (zearly) issue_z();
            scale_x(0); scale_x(1);
            if (zearly) WAIT_BAR(10); else WAIT_BAR(6);
            scale_x(2); scale_x(3);
            if (zearly) WAIT_BAR(4); else WAIT_BAR(0);
            put_x();
            if (!zearly) {
                if (hw == 3) {
#pragma unroll 1
                    for (u32 spins = 0; spins < SPIN_CAP; ++spins) {
                        ld8_issue(zf, (lane & 31) * 8, p.ws + WS_ZF + hg * 256);
                        asm volatile("s_waitcnt vmcnt(0)" : "+v"(zf)::"memory");
                        if (zseen()) break;
                    }
                    *(volatile LAS u32*)(lds + M_ZOK) = 1u;
                    asm volatile("" ::: "memory");
                } else lds_wait<0>(lds + M_ZOK, 1u);
                issue_z();
            }
            WAIT_BAR(0);
            BAR();
            store_rows();
        }
    } else {
        const int blk = (lane >> 4) & 1, q4 = (lane & 15) >> 2, pp = lane & 3;
        const int wch = 4 * wid + 2 * blk + (pp >> 1);
        u64 tag = 0;
        if (wid == 3) {
            tag = ld8_dev(p.ws + WS_EP) + 0x9E3779B97F4A7C15ull;
            asm volatile("" : "+v"(tag));
            if (lane == 0) {
                *(LAS u32x2*)(lds + M_TAG) = u32x2{(u32)tag, (u32)(tag >> 32)};
                *(LAS u32*)(lds + M_FULL) = 2u;
                *(LAS u32x4*)(lds + M_JOIN) = u32x4{0u, 0u, 0u, 0u};
                *(LAS u32x4*)(lds + M_OJ) = u32x4{0u, 0u, 0u, 0u};
                *(LAS u32x4*)(lds + M_ZJ) = u32x4{0u, 0u, 0u, 0u};
                *(LAS u32*)(lds + M_WRDY) = 0u;
            }
        }
        *(LAS u32*)(lds + M_FREE + wid * 256 + lane * 4) = 0u;
        u32 pfw = 0;
        if (wid == 2) asm volatile("global_load_dword %0, %1, %2" : "=v"(pfw) : "v"(lane * 128), "s"((const char*)p.wq + bid * 8192) : "memory");
        u32 pft = 0;
        if (wid == 1) {
            const char* a = lane == 0 ? (const char*)p.k + (size_t)rt * 131072 : lane == 1 ? (const char*)p.v + (size_t)rt * 131072 :
                            lane == 2 ? (const char*)p.ssmk + (size_t)bid * 16384 : lane == 3 ? (const char*)p.sk : lane == 4 ? (const char*)p.ssmd :
                            lane == 5 ? (const char*)p.wo + bid * 8192 : (const char*)p.out + (size_t)rt * 131072;
            asm volatile("global_load_dword %0, %1, off" : "=v"(pft) : "v"(a) : "memory");
        }
        if (wid == 0) {
            const u32 o = lane < 4 ? WS_UT + hg * 524288 + lane * 131072 : lane < 8 ? WS_ZT + hg * 524288 + (lane & 3) * 131072 :
                          lane == 8 ? WS_UT + bid * 16384 : lane == 9 ? WS_ZT + bid * 16384 : lane == 10 ? WS_O + rt * 131072 : WS_KV + (lane & 1) * 131072;
            asm volatile("global_load_dword %0, %1, %2 sc1" : "=v"(pft) : "v"(o), "s"(p.ws) : "memory");
        }
        u32 w0 = ldsb + L_QR + 4096 + 256 * (8 * h + q4) + 16 * (wch ^ ((q4 << 2) | (2 * h))) + 8 * (pp & 1);
        u32 w1 = ldsb + L_QR + 4096 + 256 * (8 * h + 4 + q4) + 16 * (wch ^ ((q4 << 2) | (2 * h + 1))) + 8 * (pp & 1);
        u32 xr[4], xs[4];
#pragma unroll
        for (int ks = 0; ks < 4; ++ks) { xr[ks] = ldsb + L_XR + r32 * 128 + 16 * ((2 * ks + h) ^ ((r32 >> 1) & 7)); xs[ks] = xr[ks] + (L_QR - L_XR); }
        asm volatile("" : "+v"(w0), "+v"(w1), "+v"(xr[0]), "+v"(xr[1]), "+v"(xr[2]), "+v"(xr[3]), "+v"(xs[0]), "+v"(xs[1]), "+v"(xs[2]), "+v"(xs[3]));
        u32 kvstat = 0;
        {
            f32x4 kv = {0.f, 0.f, 0.f, 0.f};
            const int g = lane >> 4, c15 = lane & 15;
            u32 xr0 = ldsb + L_XR + (wid & 1) * 2048 + c15 * 128 + 16 * (g ^ (c15 >> 1)), xr1 = ldsb + L_XR + (wid & 1) * 2048 + c15 * 128 + 16 * ((4 + g) ^ (c15 >> 1));
            u32 wr = wid < 2 ? ldsb + L_WKV + 3 * 8192 + (8 * g + q4) * 32 + 8 * pp : ldsb + M_GW + g * 16;
#pragma unroll 1
            for (int sg = 0; sg < 4; ++sg) {
                BAR();
                if (wid < 2) {
                    if (cg < 2) {
    asm volatile(
        "ds_read_b64_tr_b16 v[132:133], %[w] offset:0\n\t"
        "ds_read_b64_tr_b16 v[134:135], %[w] offset:128\n\t"
        "ds_read_b128 v[164:167], %[x0] offset:0\n\t"
        "ds_read_b64_tr_b16 v[136:137], %[w] offset:1024\n\t"
        "ds_read_b64_tr_b16 v[138:139], %[w] offset:1152\n\t"
        "ds_read_b128 v[168:171], %[x1] offset:0\n\t"
        "ds_read_b64_tr_b16 v[140:141], %[w] offset:2048\n\t"
        "ds_read_b64_tr_b16 v[142:143], %[w] offset:2176\n\t"
        "ds_read_b128 v[172:175], %[x0] offset:4096\n\t"
        "ds_read_b64_tr_b16 v[144:145], %[w] offset:3072\n\t"
        "ds_read_b64_tr_b16 v[146:147], %[w] offset:3200\n\t"
        "ds_read_b128 v[176:179], %[x1] offset:4096\n\t"
        "s_waitcnt lgkmcnt(9)\n\t"
        "v_mfma_f32_16x16x32_bf16 %[acc], v[132:135], v[164:167], %[acc]\n\t"
        "ds_read_b64_tr_b16 v[148:149], %[w] offset:4096\n\t"
        "ds_read_b64_tr_b16 v[150:151], %[w] offset:4224\n\t"
        "ds_read_b128 v[180:183], %[x0] offset:8192\n\t"
        "s_waitcnt lgkmcnt(9)\n\t"
        "v_mfma_f32_16x16x32_bf16 %[acc], v[136:139], v[168:171], %[acc]\n\t"
        "ds_read_b64_tr_b16 v[152:153], %[w] offset:5120\n\t"
        "ds_read_b64_tr_b16 v[154:155], %[w] offset:5248\n\t"
        "ds_read_b128 v[184:187], %[x1] offset:8192\n\t"
        "s_waitcnt lgkmcnt(9)\n\t"
        "v_mfma_f32_16x16x32_bf16 %[acc], v[140:143], v[172:175], %[acc]\n\t"
        "ds_read_b64_tr_b16 v[156:157], %[w] offset:6144\n\t"
        "ds_read_b64_tr_b16 v[158:159], %[w] offset:6272\n\t"
        "ds_read_b128 v[188:191], %[x0] offset:12288\n\t"
        "s_waitcnt lgkmcnt(9)\n\t"
        "v_mfma_f32_16x16x32_bf16 %[acc], v[144:147], v[176:179], %[acc]\n\t"
        "ds_read_b64_tr_b16 v[160:161], %[w] offset:7168\n\t"
        "ds_read_b64_tr_b16 v[162:163], %[w] offset:7296\n\t"
        "ds_read_b128 v[192:195], %[x1] offset:12288\n\t"
        "s_waitcnt lgkmcnt(9)\n\t"
        "v_mfma_f32_16x16x32_bf16 %[acc], v[148:151], v[180:183], %[acc]\n\t"
        "s_waitcnt lgkmcnt(6)\n\t"
        "v_mfma_f32_16x16x32_bf16 %[acc], v[152:155], v[184:187], %[acc]\n\t"
        "s_waitcnt lgkmcnt(3)\n\t"
        "v_mfma_f32_16x16x32_bf16 %[acc], v[156:159], v[188:191], %[acc]\n\t"
        "s_waitcnt lgkmcnt(0)\n\t"
        "v_mfma_f32_16x16x32_bf16 %[acc], v[160:163], v[192:195], %[acc]\n\t"
        "v_add_u32 %[x0], 0x4000, %[x0]\n\t"
        "v_add_u32 %[x1], 0x4000, %[x1]\n\t"
        "v_subrev_u32 %[w], 0x2000, %[w]\n\t"
        "s_nop 4\n\t"
        : [acc] "+&v"(kv), [w] "+&v"(wr), [x0] "+&v"(xr0), [x1] "+&v"(xr1) :: "memory", "v132", "v133", "v134", "v135", "v136", "v137", "v138", "v139", "v140", "v141", "v142", "v143", "v144", "v145", "v146", "v147", "v148", "v149", "v150", "v151", "v152", "v153", "v154", "v155", "v156", "v157", "v158", "v159", "v160", "v161", "v162", "v163", "v164", "v165", "v166", "v167", "v168", "v169", "v170", "v171", "v172", "v173", "v174", "v175", "v176", "v177", "v178", "v179", "v180", "v181", "v182", "v183", "v184", "v185", "v186", "v187", "v188", "v189", "v190", "v191", "v192", "v193", "v194", "v195");
                    } else {
    asm volatile(
        "ds_read_b64_tr_b16 v[132:133], %[w] offset:0\n\t"
        "ds_read_b64_tr_b16 v[134:135], %[w] offset:128\n\t"
        "ds_read_b128 v[164:167], %[x0] offset:0\n\t"
        "ds_read_b64_tr_b16 v[136:137], %[w] offset:1024\n\t"
        "ds_read_b64_tr_b16 v[138:139], %[w] offset:1152\n\t"
        "ds_read_b128 v[168:171], %[x1] offset:0\n\t"
        "ds_read_b64_tr_b16 v[140:141], %[w] offset:2048\n\t"
        "ds_read_b64_tr_b16 v[142:143], %[w] offset:2176\n\t"
        "ds_read_b128 v[172:175], %[x0] offset:4096\n\t"
        "ds_read_b64_tr_b16 v[144:145], %[w] offset:3072\n\t"
        "ds_read_b64_tr_b16 v[146:147], %[w] offset:3200\n\t"
        "ds_read_b128 v[176:179], %[x1] offset:4096\n\t"
        "s_waitcnt lgkmcnt(9)\n\t"
        "v_mfma_f32_16x16x32_bf16 %[acc], v[164:167], v[132:135], %[acc]\n\t"
        "ds_read_b64_tr_b16 v[148:149], %[w] offset:4096\n\t"
        "ds_read_b64_tr_b16 v[150:151], %[w] offset:4224\n\t"
        "ds_read_b128 v[180:183], %[x0] offset:8192\n\t"
        "s_waitcnt lgkmcnt(9)\n\t"
        "v_mfma_f32_16x16x32_bf16 %[acc], v[168:171], v[136:139], %[acc]\n\t"
        "ds_read_b64_tr_b16 v[152:153], %[w] offset:5120\n\t"
        "ds_read_b64_tr_b16 v[154:155], %[w] offset:5248\n\t"
        "ds_read_b128 v[184:187], %[x1] offset:8192\n\t"
        "s_waitcnt lgkmcnt(9)\n\t"
        "v_mfma_f32_16x16x32_bf16 %[acc], v[172:175], v[140:143], %[acc]\n\t"
        "ds_read_b64_tr_b16 v[156:157], %[w] offset:6144\n\t"
        "ds_read_b64_tr_b16 v[158:159], %[w] offset:6272\n\t"
        "ds_read_b128 v[188:191], %[x0] offset:12288\n\t"
        "s_waitcnt lgkmcnt(9)\n\t"
        "v_mfma_f32_16x16x32_bf16 %[acc], v[176:179], v[144:147], %[acc]\n\t"
        "ds_read_b64_tr_b16 v[160:161], %[w] offset:7168\n\t"
        "ds_read_b64_tr_b16 v[162:163], %[w] offset:7296\n\t"
        "ds_read_b128 v[192:195], %[x1] offset:12288\n\t"
        "s_waitcnt lgkmcnt(9)\n\t"
        "v_mfma_f32_16x16x32_bf16 %[acc], v[180:183], v[148:151], %[acc]\n\t"
        "s_waitcnt lgkmcnt(6)\n\t"
        "v_mfma_f32_16x16x32_bf16 %[acc], v[184:187], v[152:155], %[acc]\n\t"
        "s_waitcnt lgkmcnt(3)\n\t"
        "v_mfma_f32_16x16x32_bf16 %[acc], v[188:191], v[156:159], %[acc]\n\t"
        "s_waitcnt lgkmcnt(0)\n\t"
        "v_mfma_f32_16x16x32_bf16 %[acc], v[192:195], v[160:163], %[acc]\n\t"
        "v_add_u32 %[x0], 0x4000, %[x0]\n\t"
        "v_add_u32 %[x1], 0x4000, %[x1]\n\t"
        "v_subrev_u32 %[w], 0x2000, %[w]\n\t"
        "s_nop 4\n\t"
        : [acc] "+&v"(kv), [w] "+&v"(wr), [x0] "+&v"(xr0), [x1] "+&v"(xr1) :: "memory", "v132", "v133", "v134", "v135", "v136", "v137", "v138", "v139", "v140", "v141", "v142", "v143", "v144", "v145", "v146", "v147", "v148", "v149", "v150", "v151", "v152", "v153", "v154", "v155", "v156", "v157", "v158", "v159", "v160", "v161", "v162", "v163", "v164", "v165", "v166", "v167", "v168", "v169", "v170", "v171", "v172", "v173", "v174", "v175", "v176", "v177", "v178", "v179", "v180", "v181", "v182", "v183", "v184", "v185", "v186", "v187", "v188", "v189", "v190", "v191", "v192", "v193", "v194", "v195");
                    }
                } else if (cg == 0) {
    asm volatile(
        "ds_read_b128 v[132:135], %[w] offset:0\n\t"
        "ds_read_b128 v[164:167], %[x0] offset:0\n\t"
        "ds_read_b128 v[136:139], %[w] offset:64\n\t"
        "ds_read_b128 v[168:171], %[x1] offset:0\n\t"
        "ds_read_b128 v[140:143], %[w] offset:128\n\t"
        "ds_read_b128 v[172:175], %[x0] offset:4096\n\t"
        "ds_read_b128 v[144:147], %[w] offset:192\n\t"
        "ds_read_b128 v[176:179], %[x1] offset:4096\n\t"
        "s_waitcnt lgkmcnt(6)\n\t"
        "v_mfma_f32_16x16x32_bf16 %[acc], v[164:167], v[132:135], %[acc]\n\t"
        "ds_read_b128 v[148:151], %[w] offset:256\n\t"
        "ds_read_b128 v[180:183], %[x0] offset:8192\n\t"
        "s_waitcnt lgkmcnt(6)\n\t"
        "v_mfma_f32_16x16x32_bf16 %[acc], v[168:171], v[136:139], %[acc]\n\t"
        "ds_read_b128 v[152:155], %[w] offset:320\n\t"
        "ds_read_b128 v[184:187], %[x1] offset:8192\n\t"
        "s_waitcnt lgkmcnt(6)\n\t"
        "v_mfma_f32_16x16x32_bf16 %[acc], v[172:175], v[140:143], %[acc]\n\t"
        "ds_read_b128 v[156:159], %[w] offset:384\n\t"
        "ds_read_b128 v[188:191], %[x0] offset:12288\n\t"
        "s_waitcnt lgkmcnt(6)\n\t"
        "v_mfma_f32_16x16x32_bf16 %[acc], v[176:179], v[144:147], %[acc]\n\t"
        "ds_read_b128 v[160:163], %[w] offset:448\n\t"
        "ds_read_b128 v[192:195], %[x1] offset:12288\n\t"
        "s_waitcnt lgkmcnt(6)\n\t"
        "v_mfma_f32_16x16x32_bf16 %[acc], v[180:183], v[148:151], %[acc]\n\t"
        "s_waitcnt lgkmcnt(4)\n\t"
        "v_mfma_f32_16x16x32_bf16 %[acc], v[184:187], v[152:155], %[acc]\n\t"
        "s_waitcnt lgkmcnt(2)\n\t"
        "v_mfma_f32_16x16x32_bf16 %[acc], v[188:191], v[156:159], %[acc]\n\t"
        "s_waitcnt lgkmcnt(0)\n\t"
        "v_mfma_f32_16x16x32_bf16 %[acc], v[192:195], v[160:163], %[acc]\n\t"
        "v_add_u32 %[x0], 0x4000, %[x0]\n\t"
        "v_add_u32 %[x1], 0x4000, %[x1]\n\t"
        "v_add_u32 %[w], 0x200, %[w]\n\t"
        "s_nop 4\n\t"
        : [acc] "+&v"(kv), [w] "+&v"(wr), [x0] "+&v"(xr0), [x1] "+&v"(xr1) :: "memory", "v132", "v133", "v134", "v135", "v136", "v137", "v138", "v139", "v140", "v141", "v142", "v143", "v144", "v145", "v146", "v147", "v148", "v149", "v150", "v151", "v152", "v153", "v154", "v155", "v156", "v157", "v158", "v159", "v160", "v161", "v162", "v163", "v164", "v165", "v166", "v167", "v168", "v169", "v170", "v171", "v172", "v173", "v174", "v175", "v176", "v177", "v178", "v179", "v180", "v181", "v182", "v183", "v184", "v185", "v186", "v187", "v188", "v189", "v190", "v191", "v192", "v193", "v194", "v195");
                }
            }
            asm volatile("s_waitcnt vmcnt(0)" : "+v"(pfw), "+v"(pft), "+v"(pfc)::"memory");
            if (wid >= 2 && cg == 0 && c15 == 0) {
                u32x4 gv;
#pragma unroll
                for (int i = 0; i < 4; ++i) gv[i] = __float_as_uint(__builtin_amdgcn_rcpf(1.f + __expf(-kv[i])));
                st16_dev((u32)(64 * rt + 32 * par + 16 * (wid & 1) + 4 * g) * 4, p.ws + WS_G, gv);
            }
            if (wid < 2) {
                if (cg < 2) kv *= QSCALE;
                const u32 o = cg < 2 ? (u32)(cg * 1024 + ((g & 1) * 32 + 16 * wid + c15) * 16 + (g >> 1) * 8)
                                     : (u32)(2048 + wid * 1024 + ((g & 1) * 32 + 16 * (cg - 2) + c15) * 16 + (g >> 1) * 8);
                st8_dev(o, p.ws + WS_KV + (size_t)(2 * rt + par) * 4096, u32x2{pk2(kv[0], kv[1]), pk2(kv[2], kv[3])});
                if (cg < 2) {
                    const float m0 = swap32sum(swap16sum(kv[0] * kv[0] + kv[1] * kv[1] + kv[2] * kv[2] + kv[3] * kv[3]));
                    kvstat = __float_as_uint(m0) & 0x7fffffffu;
                } else {
#pragma unroll
                    for (int i = 0; i < 4; ++i) kvstat = max(kvstat, __float_as_uint(kv[i]) & 0x7fffffffu);
                }
                kvstat = wave_umax(kvstat);
            }
        }
        {
            const u32x2 tg2 = *(const LAS u32x2*)(lds + M_TAG);
            tag = ((u64)rfl(tg2[1]) << 32) | rfl(tg2[0]);
        }
        f32x16 a0, a1;
        {
            u32x4 f0, f1, f2, f3;
            auto poll_issue = [&]() {
                ld16_issue(f0, vl16, p.ws + WS_KVF); ld16_issue(f1, vl16, p.ws + WS_KVF + 1024);
                ld16_issue(f2, vl16, p.ws + WS_KVF + 2048); ld16_issue(f3, vl16, p.ws + WS_KVF + 3072);
            };
            u32x16 fa0, fa1, fa2;
            u32 jj = 0, dd;
            const u32 dq = QSLOT, dw = (u32)(-3 * QSLOT);
    asm volatile(
        "s_waitcnt lgkmcnt(0)\n\t"
        "s_barrier\n\t"
        "ds_read_b64_tr_b16 v[132:133], %[w0] offset:0\n\t"
        "ds_read_b64_tr_b16 v[134:135], %[w1] offset:0\n\t"
        "ds_read_b128 v[148:151], %[r0]\n\t"
        "ds_read_b128 v[164:167], %[s0]\n\t"
        "ds_read_b64_tr_b16 v[136:137], %[w0] offset:4096\n\t"
        "ds_read_b64_tr_b16 v[138:139], %[w1] offset:4096\n\t"
        "ds_read_b128 v[152:155], %[r1]\n\t"
        "ds_read_b128 v[168:171], %[s1]\n\t"
        "ds_read_b64_tr_b16 v[140:141], %[w0] offset:8192\n\t"
        "ds_read_b64_tr_b16 v[142:143], %[w1] offset:8192\n\t"
        "ds_read_b128 v[156:159], %[r2]\n\t"
        "ds_read_b128 v[172:175], %[s2]\n\t"
        "ds_read_b64_tr_b16 v[144:145], %[w0] offset:12288\n\t"
        "ds_read_b64_tr_b16 v[146:147], %[w1] offset:12288\n\t"
        "ds_read_b128 v[160:163], %[r3]\n\t"
        "ds_read_b128 v[176:179], %[s3]\n\t"
        "s_waitcnt lgkmcnt(12)\n\t"
        "v_mfma_f32_32x32x16_bf16 v[100:115], v[132:135], v[148:151], 0\n\t"
        "s_add_u32 %[j], %[j], 1\n\t"
        "s_and_b32 %[d], %[j], 3\n\t"
        "s_cselect_b32 %[d], %[dq], %[dw]\n\t"
        "v_add_u32 %[r0], 0x1000, %[r0]\n\t"
        "v_add_u32 %[r1], 0x1000, %[r1]\n\t"
        "v_add_u32 %[r2], 0x1000, %[r2]\n\t"
        "v_add_u32 %[r3], 0x1000, %[r3]\n\t"
        "v_mfma_f32_32x32x16_bf16 v[116:131], v[132:135], v[164:167], 0\n\t"
        "v_add_u32 %[w0], %[d], %[w0]\n\t"
        "v_add_u32 %[w1], %[d], %[w1]\n\t"
        "v_add_u32 %[s0], %[d], %[s0]\n\t"
        "v_add_u32 %[s1], %[d], %[s1]\n\t"
        "v_add_u32 %[s2], %[d], %[s2]\n\t"
        "v_add_u32 %[s3], %[d], %[s3]\n\t"
        "s_waitcnt lgkmcnt(0)\n\t"
        "s_nop 7\n\t"
        : "=&{v[100:115]}"(a0), "=&{v[116:131]}"(a1), "=&{v[132:147]}"(fa0), "=&{v[148:163]}"(fa1), "=&{v[164:179]}"(fa2), [w0] "+&v"(w0), [w1] "+&v"(w1), [r0] "+&v"(xr[0]), [r1] "+&v"(xr[1]), [r2] "+&v"(xr[2]), [r3] "+&v"(xr[3]), [s0] "+&v"(xs[0]), [s1] "+&v"(xs[1]), [s2] "+&v"(xs[2]), [s3] "+&v"(xs[3]), [j] "+&s"(jj), [d] "=&s"(dd)
        : [dq] "s"(dq), [dw] "s"(dw) : "memory", "scc");
            bool pend = true;
            u32 gl;
#pragma unroll 1
            for (int pr = 0; pr < 7; ++pr) {
                u32 nn = pend ? 0 : (wid == 3 ? (pr < 4 ? 4 - pr : 0) : 6 - pr);
                jj = 2 * pr + 1;
                pr += nn;
    asm volatile(
        "LOOP_%=:\n\t"
        "s_waitcnt lgkmcnt(0)\n\t"
        "s_barrier\n\t"
        "v_mfma_f32_32x32x16_bf16 v[100:115], v[136:139], v[152:155], v[100:115]\n\t"
        "ds_read_b64_tr_b16 v[180:181], %[w0] offset:0\n\t"
        "ds_read_b64_tr_b16 v[182:183], %[w1] offset:0\n\t"
        "ds_read_b128 v[196:199], %[r0]\n\t"
        "v_mfma_f32_32x32x16_bf16 v[116:131], v[136:139], v[168:171], v[116:131]\n\t"
        "ds_read_b128 v[212:215], %[s0]\n\t"
        "ds_read_b64_tr_b16 v[184:185], %[w0] offset:4096\n\t"
        "ds_read_b64_tr_b16 v[186:187], %[w1] offset:4096\n\t"
        "v_mfma_f32_32x32x16_bf16 v[100:115], v[140:143], v[156:159], v[100:115]\n\t"
        "ds_read_b128 v[200:203], %[r1]\n\t"
        "ds_read_b128 v[216:219], %[s1]\n\t"
        "v_mfma_f32_32x32x16_bf16 v[116:131], v[140:143], v[172:175], v[116:131]\n\t"
        "ds_read_b64_tr_b16 v[188:189], %[w0] offset:8192\n\t"
        "ds_read_b64_tr_b16 v[190:191], %[w1] offset:8192\n\t"
        "ds_read_b128 v[204:207], %[r2]\n\t"
        "v_mfma_f32_32x32x16_bf16 v[100:115], v[144:147], v[160:163], v[100:115]\n\t"
        "ds_read_b128 v[220:223], %[s2]\n\t"
        "ds_read_b64_tr_b16 v[192:193], %[w0] offset:12288\n\t"
        "ds_read_b64_tr_b16 v[194:195], %[w1] offset:12288\n\t"
        "v_mfma_f32_32x32x16_bf16 v[116:131], v[144:147], v[176:179], v[116:131]\n\t"
        "ds_read_b128 v[208:211], %[r3]\n\t"
        "ds_read_b128 v[224:227], %[s3]\n\t"
        "s_waitcnt lgkmcnt(12)\n\t"
        "v_mfma_f32_32x32x16_bf16 v[100:115], v[180:183], v[196:199], v[100:115]\n\t"
        "s_add_u32 %[j], %[j], 1\n\t"
        "s_and_b32 %[d], %[j], 3\n\t"
        "s_cselect_b32 %[d], %[dq], %[dw]\n\t"
        "v_add_u32 %[r0], 0x1000, %[r0]\n\t"
        "v_add_u32 %[r1], 0x1000, %[r1]\n\t"
        "v_add_u32 %[r2], 0x1000, %[r2]\n\t"
        "v_add_u32 %[r3], 0x1000, %[r3]\n\t"
        "v_mfma_f32_32x32x16_bf16 v[116:131], v[180:183], v[212:215], v[116:131]\n\t"
        "v_add_u32 %[w0], %[d], %[w0]\n\t"
        "v_add_u32 %[w1], %[d], %[w1]\n\t"
        "v_add_u32 %[s0], %[d], %[s0]\n\t"
        "v_add_u32 %[s1], %[d], %[s1]\n\t"
        "v_add_u32 %[s2], %[d], %[s2]\n\t"
        "v_add_u32 %[s3], %[d], %[s3]\n\t"
        "s_waitcnt lgkmcnt(0)\n\t"
        "s_barrier\n\t"
        "v_mfma_f32_32x32x16_bf16 v[100:115], v[184:187], v[200:203], v[100:115]\n\t"
        "ds_read_b64_tr_b16 v[132:133], %[w0] offset:0\n\t"
        "ds_read_b64_tr_b16 v[134:135], %[w1] offset:0\n\t"
        "ds_read_b128 v[148:151], %[r0]\n\t"
        "v_mfma_f32_32x32x16_bf16 v[116:131], v[184:187], v[216:219], v[116:131]\n\t"
        "ds_read_b128 v[164:167], %[s0]\n\t"
        "ds_read_b64_tr_b16 v[136:137], %[w0] offset:4096\n\t"
        "ds_read_b64_tr_b16 v[138:139], %[w1] offset:4096\n\t"
        "v_mfma_f32_32x32x16_bf16 v[100:115], v[188:191], v[204:207], v[100:115]\n\t"
        "ds_read_b128 v[152:155], %[r1]\n\t"
        "ds_read_b128 v[168:171], %[s1]\n\t"
        "v_mfma_f32_32x32x16_bf16 v[116:131], v[188:191], v[220:223], v[116:131]\n\t"
        "ds_read_b64_tr_b16 v[140:141], %[w0] offset:8192\n\t"
        "ds_read_b64_tr_b16 v[142:143], %[w1] offset:8192\n\t"
        "ds_read_b128 v[156:159], %[r2]\n\t"
        "v_mfma_f32_32x32x16_bf16 v[100:115], v[192:195], v[208:211], v[100:115]\n\t"
        "ds_read_b128 v[172:175], %[s2]\n\t"
        "ds_read_b64_tr_b16 v[144:145], %[w0] offset:12288\n\t"
        "ds_read_b64_tr_b16 v[146:147], %[w1] offset:12288\n\t"
        "v_mfma_f32_32x32x16_bf16 v[116:131], v[192:195], v[224:227], v[116:131]\n\t"
        "ds_read_b128 v[160:163], %[r3]\n\t"
        "ds_read_b128 v[176:179], %[s3]\n\t"
        "s_waitcnt lgkmcnt(12)\n\t"
        "v_mfma_f32_32x32x16_bf16 v[100:115], v[132:135], v[148:151], v[100:115]\n\t"
        "s_add_u32 %[j], %[j], 1\n\t"
        "s_and_b32 %[d], %[j], 3\n\t"
        "s_cselect_b32 %[d], %[dq], %[dw]\n\t"
        "v_add_u32 %[r0], 0x1000, %[r0]\n\t"
        "v_add_u32 %[r1], 0x1000, %[r1]\n\t"
        "v_add_u32 %[r2], 0x1000, %[r2]\n\t"
        "v_add_u32 %[r3], 0x1000, %[r3]\n\t"
        "v_mfma_f32_32x32x16_bf16 v[116:131], v[132:135], v[164:167], v[116:131]\n\t"
        "v_add_u32 %[w0], %[d], %[w0]\n\t"
        "v_add_u32 %[w1], %[d], %[w1]\n\t"
        "v_add_u32 %[s0], %[d], %[s0]\n\t"
        "v_add_u32 %[s1], %[d], %[s1]\n\t"
        "v_add_u32 %[s2], %[d], %[s2]\n\t"
        "v_add_u32 %[s3], %[d], %[s3]\n\t"
        "s_sub_u32 %[n], %[n], 1\n\t"
        "s_cbranch_scc0 LOOP_%=\n\t"
        "s_waitcnt lgkmcnt(0)\n\t"
        : "+{v[100:115]}"(a0), "+{v[116:131]}"(a1), "+{v[132:147]}"(fa0), "+{v[148:163]}"(fa1), "+{v[164:179]}"(fa2), [w0] "+&v"(w0), [w1] "+&v"(w1), [r0] "+&v"(xr[0]), [r1] "+&v"(xr[1]), [r2] "+&v"(xr[2]), [r3] "+&v"(xr[3]), [s0] "+&v"(xs[0]), [s1] "+&v"(xs[1]), [s2] "+&v"(xs[2]), [s3] "+&v"(xs[3]), [j] "+&s"(jj), [d] "=&s"(dd), [n] "+&s"(nn)
        : [dq] "s"(dq), [dw] "s"(dw) : "memory", "scc", "v180", "v181", "v182", "v183", "v184", "v185", "v186", "v187", "v188", "v189", "v190", "v191", "v192", "v193", "v194", "v195", "v196", "v197", "v198", "v199", "v200", "v201", "v202", "v203", "v204", "v205", "v206", "v207", "v208", "v209", "v210", "v211", "v212", "v213", "v214", "v215", "v216", "v217", "v218", "v219", "v220", "v221", "v222", "v223", "v224", "v225", "v226", "v227");
                if (pend && (pr >= 3 || vm_idle())) {
                    WAIT_VM0();
                    pend = false;
                    u32 cnt, st;
                    asm volatile("s_mov_b64 exec, 1\n\tds_max_u32 %2, %3 offset:4\n\tds_add_rtn_u32 %0, %2, %4\n\tds_read_b32 %1, %2 offset:4\n\ts_mov_b64 exec, -1\n\ts_waitcnt lgkmcnt(0)"
                                 : "=&v"(cnt), "=&v"(st) : "v"(ldsb + M_JOIN), "v"(kvstat), "v"(1u) : "memory");
                    if (rfl(cnt) == 3u && lane == 0) st16_dev((u32)((rt * 8 + hg) * 16), p.ws + WS_KVF, u32x4{(u32)tag, st, (u32)(tag >> 32), st});
                }
                if (wid == 3 && pr == 4) poll_issue();
                if (wid == 3 && pr == 5) {
#pragma unroll 1
                    for (u32 spins = 0; spins < SPIN_CAP; ++spins) {
                        asm volatile("s_waitcnt vmcnt(0)" : "+v"(f0), "+v"(f1), "+v"(f2), "+v"(f3)::"memory");
                        const u32 lo = (u32)tag, hi = (u32)(tag >> 32);
                        const bool ok = f0[0] == lo && f0[2] == hi && f1[0] == lo && f1[2] == hi && f2[0] == lo && f2[2] == hi && f3[0] == lo && f3[2] == hi;
                        if (__all(ok)) break;
                        poll_issue();
                    }
                    asm volatile("s_waitcnt vmcnt(0)" : "+v"(f0), "+v"(f1), "+v"(f2), "+v"(f3)::"memory");
                    if (bid == 0 && lane == 0) st8g_dev(p.ws + WS_EP, tag);
                    asm volatile("global_load_dword %0, %1, %2 sc1" : "=v"(gl) : "v"(lane * 4), "s"(p.ws + WS_G + rt * 256) : "memory");
                }
            }
    asm volatile(
        "s_waitcnt lgkmcnt(0)\n\t"
        "s_barrier\n\t"
        "v_mfma_f32_32x32x16_bf16 v[100:115], v[136:139], v[152:155], v[100:115]\n\t"
        "ds_read_b64_tr_b16 v[180:181], %[w0] offset:0\n\t"
        "ds_read_b64_tr_b16 v[182:183], %[w1] offset:0\n\t"
        "ds_read_b128 v[196:199], %[r0]\n\t"
        "v_mfma_f32_32x32x16_bf16 v[116:131], v[136:139], v[168:171], v[116:131]\n\t"
        "ds_read_b128 v[212:215], %[s0]\n\t"
        "ds_read_b64_tr_b16 v[184:185], %[w0] offset:4096\n\t"
        "ds_read_b64_tr_b16 v[186:187], %[w1] offset:4096\n\t"
        "v_mfma_f32_32x32x16_bf16 v[100:115], v[140:143], v[156:159], v[100:115]\n\t"
        "ds_read_b128 v[200:203], %[r1]\n\t"
        "ds_read_b128 v[216:219], %[s1]\n\t"
        "v_mfma_f32_32x32x16_bf16 v[116:131], v[140:143], v[172:175], v[116:131]\n\t"
        "ds_read_b64_tr_b16 v[188:189], %[w0] offset:8192\n\t"
        "ds_read_b64_tr_b16 v[190:191], %[w1] offset:8192\n\t"
        "ds_read_b128 v[204:207], %[r2]\n\t"
        "v_mfma_f32_32x32x16_bf16 v[100:115], v[144:147], v[160:163], v[100:115]\n\t"
        "ds_read_b128 v[220:223], %[s2]\n\t"
        "ds_read_b64_tr_b16 v[192:193], %[w0] offset:12288\n\t"
        "ds_read_b64_tr_b16 v[194:195], %[w1] offset:12288\n\t"
        "v_mfma_f32_32x32x16_bf16 v[116:131], v[144:147], v[176:179], v[116:131]\n\t"
        "ds_read_b128 v[208:211], %[r3]\n\t"
        "ds_read_b128 v[224:227], %[s3]\n\t"
        "s_waitcnt lgkmcnt(12)\n\t"
        "v_mfma_f32_32x32x16_bf16 v[100:115], v[180:183], v[196:199], v[100:115]\n\t"
        "v_mfma_f32_32x32x16_bf16 v[116:131], v[180:183], v[212:215], v[116:131]\n\t"
        "s_waitcnt lgkmcnt(0)\n\t"
        "v_mfma_f32_32x32x16_bf16 v[100:115], v[184:187], v[200:203], v[100:115]\n\t"
        "v_mfma_f32_32x32x16_bf16 v[116:131], v[184:187], v[216:219], v[116:131]\n\t"
        "v_mfma_f32_32x32x16_bf16 v[100:115], v[188:191], v[204:207], v[100:115]\n\t"
        "v_mfma_f32_32x32x16_bf16 v[116:131], v[188:191], v[220:223], v[116:131]\n\t"
        "v_mfma_f32_32x32x16_bf16 v[100:115], v[192:195], v[208:211], v[100:115]\n\t"
        "v_mfma_f32_32x32x16_bf16 v[116:131], v[192:195], v[224:227], v[116:131]\n\t"
        "s_nop 15\n\t"
        : "+{v[100:115]}"(a0), "+{v[116:131]}"(a1), "+{v[132:147]}"(fa0), "+{v[148:163]}"(fa1), "+{v[164:179]}"(fa2), [w0] "+&v"(w0), [w1] "+&v"(w1), [r0] "+&v"(xr[0]), [r1] "+&v"(xr[1]), [r2] "+&v"(xr[2]), [r3] "+&v"(xr[3]), [s0] "+&v"(xs[0]), [s1] "+&v"(xs[1]), [s2] "+&v"(xs[2]), [s3] "+&v"(xs[3]) :: "memory", "v180", "v181", "v182", "v183", "v184", "v185", "v186", "v187", "v188", "v189", "v190", "v191", "v192", "v193", "v194", "v195", "v196", "v197", "v198", "v199", "v200", "v201", "v202", "v203", "v204", "v205", "v206", "v207", "v208", "v209", "v210", "v211", "v212", "v213", "v214", "v215", "v216", "v217", "v218", "v219", "v220", "v221", "v222", "v223", "v224", "v225", "v226", "v227");
            if (wid == 3) { asm volatile("s_waitcnt vmcnt(0)" : "+v"(gl)::"memory"); *(LAS u32*)(lds + M_G + lane * 4) = gl; }
        }
        const f32x16 qa0 = a0, qa1 = a1;
        const bf16x8 qx0 = pack8(qa0, 0), qx1 = pack8(qa0, 8), qy0 = pack8(qa1, 0), qy1 = pack8(qa1, 8);
        float n0 = 0.f, n1 = 0.f;
#pragma unroll
        for (int i = 0; i < 4; ++i) {
            const u32 a = __builtin_bit_cast(u32x4, qx0)[i], b = __builtin_bit_cast(u32x4, qx1)[i], c = __builtin_bit_cast(u32x4, qy0)[i], d = __builtin_bit_cast(u32x4, qy1)[i];
            n0 = dot2(b, b, dot2(a, a, n0)); n1 = dot2(d, d, dot2(c, c, n1));
        }
        n0 = swap32sum(n0); n1 = swap32sum(n1);
        const float nq = fmaxf(n0, n1) + (n0 + n1) * 0.f;
        BAR();
        const bool fast = __all(nq * *(const LAS float*)(lds + M_K2) <= SAFE_T2);

        f32x16 ox, oy;
        float lix, liy;
        if (fast) {
            const u32 o1 = (lane & 1) ? 0u : 0x3f803f80u, o2 = (lane & 1) ? 0x3f803f80u : 0u;
            const u32x4 ones = {o1, o1, o2, o2};
            const u32 idx = (lane & 2) ? 0xEEEEEEEEu : 0x44444444u;
            const u32 a0 = ldsb + L_KV + vl16, af = ldsb + M_FULL, aw = ldsb + M_FREE + wid * 256 + lane * 4, one = 1u;
            u32 aa = a0, ab = a0 + 16384, need = 2u, no = 16384u;
            f32x4 lx, ly;
    asm volatile(
        "ds_read_b128 v[164:167], %[aa] offset:0\n\t"
        "ds_read_b128 v[168:171], %[aa] offset:1024\n\t"
        "ds_read_b128 v[148:151], %[aa] offset:4096\n\t"
        "ds_read_b128 v[152:155], %[aa] offset:5120\n\t"
        "v_mov_b32 v132, 0\n\t"
        "v_mov_b32 v133, 0\n\t"
        "v_mov_b32 v134, 0\n\t"
        "v_mov_b32 v135, 0\n\t"
        "v_mov_b32 v136, 0\n\t"
        "v_mov_b32 v137, 0\n\t"
        "v_mov_b32 v138, 0\n\t"
        "v_mov_b32 v139, 0\n\t"
        "v_mov_b32 v140, 0\n\t"
        "v_mov_b32 v141, 0\n\t"
        "v_mov_b32 v142, 0\n\t"
        "v_mov_b32 v143, 0\n\t"
        "v_mov_b32 v144, 0\n\t"
        "v_mov_b32 v145, 0\n\t"
        "v_mov_b32 v146, 0\n\t"
        "v_mov_b32 v147, 0\n\t"
        "v_mov_b32 v172, 0\n\t"
        "v_mov_b32 v173, 0\n\t"
        "v_mov_b32 v174, 0\n\t"
        "v_mov_b32 v175, 0\n\t"
        "v_mov_b32 v176, 0\n\t"
        "v_mov_b32 v177, 0\n\t"
        "v_mov_b32 v178, 0\n\t"
        "v_mov_b32 v179, 0\n\t"
        "s_waitcnt lgkmcnt(2)\n\t"
        "v_mfma_f32_32x32x16_bf16 v[180:195], v[164:167], v[100:103], 0\n\t"
        "v_mfma_f32_32x32x16_bf16 v[180:195], v[168:171], v[104:107], v[180:195]\n\t"
        "v_mfma_f32_32x32x16_bf16 v[212:227], v[172:175], v[132:135], 0\n\t"
        "v_mfma_f32_32x32x16_bf16 v[228:243], v[172:175], v[132:135], 0\n\t"
        "s_nop 7\n\t"
        "LOOP_%=:\n\t"
        "v_mfma_f32_32x32x16_bf16 v[196:211], v[164:167], v[108:111], 0\n\t"
        "ds_read_b128 v[156:159], %[aa] offset:2048\n\t"
        "v_exp_f32 v180, v180\n\t"
        "v_exp_f32 v181, v181\n\t"
        "v_exp_f32 v182, v182\n\t"
        "v_mfma_f32_32x32x16_bf16 v[196:211], v[168:171], v[112:115], v[196:211]\n\t"
        "v_cvt_pk_bf16_f32 v124, v180, v181\n\t"
        "v_exp_f32 v183, v183\n\t"
        "v_exp_f32 v184, v184\n\t"
        "v_cvt_pk_bf16_f32 v125, v182, v183\n\t"
        "v_mfma_f32_32x32x16_bf16 v[228:243], v[172:175], v[132:135], v[228:243]\n\t"
        "v_exp_f32 v185, v185\n\t"
        "v_exp_f32 v186, v186\n\t"
        "v_cvt_pk_bf16_f32 v126, v184, v185\n\t"
        "v_exp_f32 v187, v187\n\t"
        "v_smfmac_f32_16x16x64_bf16 v[144:147], v[116:119], v[132:139], v120\n\t"
        "ds_read_b128 v[160:163], %[aa] offset:3072\n\t"
        "v_cvt_pk_bf16_f32 v127, v186, v187\n\t"
        "v_exp_f32 v188, v188\n\t"
        "v_exp_f32 v189, v189\n\t"
        "v_mfma_f32_32x32x16_bf16 v[228:243], v[176:179], v[136:139], v[228:243]\n\t"
        "s_waitcnt lgkmcnt(1)\n\t"
        "v_exp_f32 v190, v190\n\t"
        "v_cvt_pk_bf16_f32 v128, v188, v189\n\t"
        "v_exp_f32 v191, v191\n\t"
        "v_exp_f32 v192, v192\n\t"
        "v_cvt_pk_bf16_f32 v129, v190, v191\n\t"
        "v_exp_f32 v193, v193\n\t"
        "v_exp_f32 v194, v194\n\t"
        "v_exp_f32 v195, v195\n\t"
        "v_cvt_pk_bf16_f32 v130, v192, v193\n\t"
        "v_cvt_pk_bf16_f32 v131, v194, v195\n\t"
        "v_mfma_f32_32x32x16_bf16 v[180:195], v[148:151], v[100:103], 0\n\t"
        "ds_read_b128 v[164:167], %[aa] offset:8192\n\t"
        "v_exp_f32 v196, v196\n\t"
        "v_exp_f32 v197, v197\n\t"
        "v_exp_f32 v198, v198\n\t"
        "v_mfma_f32_32x32x16_bf16 v[180:195], v[152:155], v[104:107], v[180:195]\n\t"
        "v_cvt_pk_bf16_f32 v132, v196, v197\n\t"
        "v_exp_f32 v199, v199\n\t"
        "v_exp_f32 v200, v200\n\t"
        "v_cvt_pk_bf16_f32 v133, v198, v199\n\t"
        "v_mfma_f32_32x32x16_bf16 v[212:227], v[156:159], v[124:127], v[212:227]\n\t"
        "s_waitcnt lgkmcnt(1)\n\t"
        "v_exp_f32 v201, v201\n\t"
        "v_exp_f32 v202, v202\n\t"
        "v_cvt_pk_bf16_f32 v134, v200, v201\n\t"
        "v_exp_f32 v203, v203\n\t"
        "v_smfmac_f32_16x16x64_bf16 v[140:143], v[116:119], v[124:131], v120\n\t"
        "ds_read_b128 v[168:171], %[aa] offset:9216\n\t"
        "v_cvt_pk_bf16_f32 v135, v202, v203\n\t"
        "v_exp_f32 v204, v204\n\t"
        "v_exp_f32 v205, v205\n\t"
        "v_mfma_f32_32x32x16_bf16 v[212:227], v[160:163], v[128:131], v[212:227]\n\t"
        "v_exp_f32 v206, v206\n\t"
        "v_cvt_pk_bf16_f32 v136, v204, v205\n\t"
        "v_exp_f32 v207, v207\n\t"
        "v_exp_f32 v208, v208\n\t"
        "v_cvt_pk_bf16_f32 v137, v206, v207\n\t"
        "v_exp_f32 v209, v209\n\t"
        "v_exp_f32 v210, v210\n\t"
        "v_exp_f32 v211, v211\n\t"
        "v_cvt_pk_bf16_f32 v138, v208, v209\n\t"
        "v_cvt_pk_bf16_f32 v139, v210, v211\n\t"
        "v_mfma_f32_32x32x16_bf16 v[196:211], v[148:151], v[108:111], 0\n\t"
        "ds_read_b128 v[172:175], %[aa] offset:6144\n\t"
        "v_exp_f32 v180, v180\n\t"
        "v_exp_f32 v181, v181\n\t"
        "v_exp_f32 v182, v182\n\t"
        "v_mfma_f32_32x32x16_bf16 v[196:211], v[152:155], v[112:115], v[196:211]\n\t"
        "ds_read_b32 v121, %[af]\n\t"
        "v_cvt_pk_bf16_f32 v124, v180, v181\n\t"
        "v_exp_f32 v183, v183\n\t"
        "v_exp_f32 v184, v184\n\t"
        "v_cvt_pk_bf16_f32 v125, v182, v183\n\t"
        "v_mfma_f32_32x32x16_bf16 v[228:243], v[156:159], v[132:135], v[228:243]\n\t"
        "v_exp_f32 v185, v185\n\t"
        "v_exp_f32 v186, v186\n\t"
        "v_cvt_pk_bf16_f32 v126, v184, v185\n\t"
        "v_exp_f32 v187, v187\n\t"
        "v_smfmac_f32_16x16x64_bf16 v[144:147], v[116:119], v[132:139], v120\n\t"
        "ds_read_b128 v[176:179], %[aa] offset:7168\n\t"
        "v_cvt_pk_bf16_f32 v127, v186, v187\n\t"
        "v_exp_f32 v188, v188\n\t"
        "v_exp_f32 v189, v189\n\t"
        "v_mfma_f32_32x32x16_bf16 v[228:243], v[160:163], v[136:139], v[228:243]\n\t"
        "s_waitcnt lgkmcnt(1)\n\t"
        "v_exp_f32 v190, v190\n\t"
        "v_cvt_pk_bf16_f32 v128, v188, v189\n\t"
        "v_exp_f32 v191, v191\n\t"
        "v_cmp_gt_u32 vcc, %[need], v121\n\t"
        "v_exp_f32 v192, v192\n\t"
        "v_cvt_pk_bf16_f32 v129, v190, v191\n\t"
        "v_exp_f32 v193, v193\n\t"
        "v_exp_f32 v194, v194\n\t"
        "v_exp_f32 v195, v195\n\t"
        "v_cvt_pk_bf16_f32 v130, v192, v193\n\t"
        "v_cvt_pk_bf16_f32 v131, v194, v195\n\t"
        "v_mfma_f32_32x32x16_bf16 v[180:195], v[164:167], v[100:103], 0\n\t"
        "ds_read_b128 v[148:151], %[aa] offset:12288\n\t"
        "v_exp_f32 v196, v196\n\t"
        "v_exp_f32 v197, v197\n\t"
        "v_exp_f32 v198, v198\n\t"
        "v_mfma_f32_32x32x16_bf16 v[180:195], v[168:171], v[104:107], v[180:195]\n\t"
        "s_cbranch_vccnz SPIN_%=\n\t"
        "BACK_%=:\n\t"
        "v_cvt_pk_bf16_f32 v132, v196, v197\n\t"
        "v_exp_f32 v199, v199\n\t"
        "v_exp_f32 v200, v200\n\t"
        "v_cvt_pk_bf16_f32 v133, v198, v199\n\t"
        "v_mfma_f32_32x32x16_bf16 v[212:227], v[172:175], v[124:127], v[212:227]\n\t"
        "s_waitcnt lgkmcnt(1)\n\t"
        "v_exp_f32 v201, v201\n\t"
        "v_exp_f32 v202, v202\n\t"
        "v_cvt_pk_bf16_f32 v134, v200, v201\n\t"
        "v_exp_f32 v203, v203\n\t"
        "v_smfmac_f32_16x16x64_bf16 v[140:143], v[116:119], v[124:131], v120\n\t"
        "ds_read_b128 v[152:155], %[aa] offset:13312\n\t"
        "v_cvt_pk_bf16_f32 v135, v202, v203\n\t"
        "v_exp_f32 v204, v204\n\t"
        "v_exp_f32 v205, v205\n\t"
        "v_mfma_f32_32x32x16_bf16 v[212:227], v[176:179], v[128:131], v[212:227]\n\t"
        "v_exp_f32 v206, v206\n\t"
        "v_cvt_pk_bf16_f32 v136, v204, v205\n\t"
        "v_exp_f32 v207, v207\n\t"
        "v_exp_f32 v208, v208\n\t"
        "v_cvt_pk_bf16_f32 v137, v206, v207\n\t"
        "v_exp_f32 v209, v209\n\t"
        "v_exp_f32 v210, v210\n\t"
        "v_exp_f32 v211, v211\n\t"
        "v_cvt_pk_bf16_f32 v138, v208, v209\n\t"
        "v_cvt_pk_bf16_f32 v139, v210, v211\n\t"
        "v_mfma_f32_32x32x16_bf16 v[196:211], v[164:167], v[108:111], 0\n\t"
        "ds_read_b128 v[156:159], %[aa] offset:10240\n\t"
        "v_exp_f32 v180, v180\n\t"
        "v_exp_f32 v181, v181\n\t"
        "v_exp_f32 v182, v182\n\t"
        "v_mfma_f32_32x32x16_bf16 v[196:211], v[168:171], v[112:115], v[196:211]\n\t"
        "s_add_u32 %[no], %[no], 0x4000\n\t"
        "v_cvt_pk_bf16_f32 v124, v180, v181\n\t"
        "v_exp_f32 v183, v183\n\t"
        "v_exp_f32 v184, v184\n\t"
        "v_cvt_pk_bf16_f32 v125, v182, v183\n\t"
        "v_mfma_f32_32x32x16_bf16 v[228:243], v[172:175], v[132:135], v[228:243]\n\t"
        "v_exp_f32 v185, v185\n\t"
        "v_exp_f32 v186, v186\n\t"
        "v_cvt_pk_bf16_f32 v126, v184, v185\n\t"
        "v_exp_f32 v187, v187\n\t"
        "v_smfmac_f32_16x16x64_bf16 v[144:147], v[116:119], v[132:139], v120\n\t"
        "ds_read_b128 v[160:163], %[aa] offset:11264\n\t"
        "v_cvt_pk_bf16_f32 v127, v186, v187\n\t"
        "v_exp_f32 v188, v188\n\t"
        "v_exp_f32 v189, v189\n\t"
        "v_mfma_f32_32x32x16_bf16 v[228:243], v[176:179], v[136:139], v[228:243]\n\t"
        "s_waitcnt lgkmcnt(1)\n\t"
        "v_exp_f32 v190, v190\n\t"
        "v_cvt_pk_bf16_f32 v128, v188, v189\n\t"
        "v_exp_f32 v191, v191\n\t"
        "v_exp_f32 v192, v192\n\t"
        "v_cvt_pk_bf16_f32 v129, v190, v191\n\t"
        "v_exp_f32 v193, v193\n\t"
        "v_exp_f32 v194, v194\n\t"
        "v_exp_f32 v195, v195\n\t"
        "v_cvt_pk_bf16_f32 v130, v192, v193\n\t"
        "v_cvt_pk_bf16_f32 v131, v194, v195\n\t"
        "v_mfma_f32_32x32x16_bf16 v[180:195], v[148:151], v[100:103], 0\n\t"
        "ds_read_b128 v[164:167], %[ab] offset:0\n\t"
        "v_exp_f32 v196, v196\n\t"
        "v_exp_f32 v197, v197\n\t"
        "v_exp_f32 v198, v198\n\t"
        "v_mfma_f32_32x32x16_bf16 v[180:195], v[152:155], v[104:107], v[180:195]\n\t"
        "s_add_u32 %[need], %[need], 1\n\t"
        "v_cvt_pk_bf16_f32 v132, v196, v197\n\t"
        "v_exp_f32 v199, v199\n\t"
        "v_exp_f32 v200, v200\n\t"
        "v_cvt_pk_bf16_f32 v133, v198, v199\n\t"
        "v_mfma_f32_32x32x16_bf16 v[212:227], v[156:159], v[124:127], v[212:227]\n\t"
        "s_waitcnt lgkmcnt(1)\n\t"
        "v_exp_f32 v201, v201\n\t"
        "v_exp_f32 v202, v202\n\t"
        "v_cvt_pk_bf16_f32 v134, v200, v201\n\t"
        "v_exp_f32 v203, v203\n\t"
        "v_smfmac_f32_16x16x64_bf16 v[140:143], v[116:119], v[124:131], v120\n\t"
        "ds_read_b128 v[168:171], %[ab] offset:1024\n\t"
        "v_cvt_pk_bf16_f32 v135, v202, v203\n\t"
        "v_exp_f32 v204, v204\n\t"
        "v_exp_f32 v205, v205\n\t"
        "v_mfma_f32_32x32x16_bf16 v[212:227], v[160:163], v[128:131], v[212:227]\n\t"
        "v_exp_f32 v206, v206\n\t"
        "v_cvt_pk_bf16_f32 v136, v204, v205\n\t"
        "v_exp_f32 v207, v207\n\t"
        "v_exp_f32 v208, v208\n\t"
        "v_cvt_pk_bf16_f32 v137, v206, v207\n\t"
        "v_exp_f32 v209, v209\n\t"
        "v_exp_f32 v210, v210\n\t"
        "v_exp_f32 v211, v211\n\t"
        "v_cvt_pk_bf16_f32 v138, v208, v209\n\t"
        "v_cvt_pk_bf16_f32 v139, v210, v211\n\t"
        "v_mfma_f32_32x32x16_bf16 v[196:211], v[148:151], v[108:111], 0\n\t"
        "ds_read_b128 v[172:175], %[aa] offset:14336\n\t"
        "v_exp_f32 v180, v180\n\t"
        "v_exp_f32 v181, v181\n\t"
        "v_exp_f32 v182, v182\n\t"
        "v_mfma_f32_32x32x16_bf16 v[196:211], v[152:155], v[112:115], v[196:211]\n\t"
        "s_and_b32 %[no], %[no], 0xffff\n\t"
        "v_cvt_pk_bf16_f32 v124, v180, v181\n\t"
        "v_exp_f32 v183, v183\n\t"
        "v_exp_f32 v184, v184\n\t"
        "v_cvt_pk_bf16_f32 v125, v182, v183\n\t"
        "v_mfma_f32_32x32x16_bf16 v[228:243], v[156:159], v[132:135], v[228:243]\n\t"
        "v_exp_f32 v185, v185\n\t"
        "v_exp_f32 v186, v186\n\t"
        "v_cvt_pk_bf16_f32 v126, v184, v185\n\t"
        "v_exp_f32 v187, v187\n\t"
        "v_smfmac_f32_16x16x64_bf16 v[144:147], v[116:119], v[132:139], v120\n\t"
        "ds_read_b128 v[176:179], %[aa] offset:15360\n\t"
        "v_cvt_pk_bf16_f32 v127, v186, v187\n\t"
        "v_exp_f32 v188, v188\n\t"
        "v_exp_f32 v189, v189\n\t"
        "v_mfma_f32_32x32x16_bf16 v[228:243], v[160:163], v[136:139], v[228:243]\n\t"
        "s_waitcnt lgkmcnt(1)\n\t"
        "v_exp_f32 v190, v190\n\t"
        "v_cvt_pk_bf16_f32 v128, v188, v189\n\t"
        "v_exp_f32 v191, v191\n\t"
        "v_exp_f32 v192, v192\n\t"
        "v_cvt_pk_bf16_f32 v129, v190, v191\n\t"
        "v_exp_f32 v193, v193\n\t"
        "v_exp_f32 v194, v194\n\t"
        "v_exp_f32 v195, v195\n\t"
        "v_cvt_pk_bf16_f32 v130, v192, v193\n\t"
        "v_cvt_pk_bf16_f32 v131, v194, v195\n\t"
        "v_mfma_f32_32x32x16_bf16 v[180:195], v[164:167], v[100:103], 0\n\t"
        "ds_read_b128 v[148:151], %[ab] offset:4096\n\t"
        "v_exp_f32 v196, v196\n\t"
        "v_exp_f32 v197, v197\n\t"
        "v_exp_f32 v198, v198\n\t"
        "v_mfma_f32_32x32x16_bf16 v[180:195], v[168:171], v[104:107], v[180:195]\n\t"
        "s_cmp_lg_u32 %[need], 18\n\t"
        "v_cvt_pk_bf16_f32 v132, v196, v197\n\t"
        "v_exp_f32 v199, v199\n\t"
        "v_exp_f32 v200, v200\n\t"
        "v_cvt_pk_bf16_f32 v133, v198, v199\n\t"
        "v_mfma_f32_32x32x16_bf16 v[212:227], v[172:175], v[124:127], v[212:227]\n\t"
        "s_waitcnt lgkmcnt(1)\n\t"
        "v_exp_f32 v201, v201\n\t"
        "v_exp_f32 v202, v202\n\t"
        "v_cvt_pk_bf16_f32 v134, v200, v201\n\t"
        "v_exp_f32 v203, v203\n\t"
        "v_smfmac_f32_16x16x64_bf16 v[140:143], v[116:119], v[124:131], v120\n\t"
        "ds_read_b128 v[152:155], %[ab] offset:5120\n\t"
        "v_cvt_pk_bf16_f32 v135, v202, v203\n\t"
        "v_exp_f32 v204, v204\n\t"
        "v_exp_f32 v205, v205\n\t"
        "v_mfma_f32_32x32x16_bf16 v[212:227], v[176:179], v[128:131], v[212:227]\n\t"
        "ds_add_u32 %[aw], %[one]\n\t"
        "v_exp_f32 v206, v206\n\t"
        "v_cvt_pk_bf16_f32 v136, v204, v205\n\t"
        "v_exp_f32 v207, v207\n\t"
        "v_mov_b32 %[aa], %[ab]\n\t"
        "v_add_u32 %[ab], %[no], %[a0]\n\t"
        "v_exp_f32 v208, v208\n\t"
        "v_cvt_pk_bf16_f32 v137, v206, v207\n\t"
        "v_exp_f32 v209, v209\n\t"
        "v_exp_f32 v210, v210\n\t"
        "v_exp_f32 v211, v211\n\t"
        "v_cvt_pk_bf16_f32 v138, v208, v209\n\t"
        "v_cvt_pk_bf16_f32 v139, v210, v211\n\t"
        "s_cbranch_scc1 LOOP_%=\n\t"
        "s_nop 1\n\t"
        "v_mfma_f32_32x32x16_bf16 v[228:243], v[172:175], v[132:135], v[228:243]\n\t"
        "v_smfmac_f32_16x16x64_bf16 v[144:147], v[116:119], v[132:139], v120\n\t"
        "v_mfma_f32_32x32x16_bf16 v[228:243], v[176:179], v[136:139], v[228:243]\n\t"
        "s_waitcnt lgkmcnt(0)\n\t"
        "s_nop 15\n\t"
        "s_branch END_%=\n\t"
        "SPIN_%=:\n\t"
        "s_sleep 1\n\t"
        "ds_read_b32 v121, %[af]\n\t"
        "s_waitcnt lgkmcnt(0)\n\t"
        "v_cmp_gt_u32 vcc, %[need], v121\n\t"
        "s_nop 4\n\t"
        "s_cbranch_vccnz SPIN_%=\n\t"
        "s_branch BACK_%=\n\t"
        "END_%=:\n\t"
        : "=&{v[212:227]}"(ox), "=&{v[228:243]}"(oy), "=&{v[140:143]}"(lx), "=&{v[144:147]}"(ly), [aa] "+&v"(aa), [ab] "+&v"(ab), [need] "+&s"(need), [no] "+&s"(no)
        : "{v[100:103]}"(qx0), "{v[104:107]}"(qx1), "{v[108:111]}"(qy0), "{v[112:115]}"(qy1), "{v[116:119]}"(ones), "{v120}"(idx), [a0] "v"(a0), [af] "v"(af), [aw] "v"(aw), [one] "v"(one)
        : "memory", "scc", "vcc", "v121", "v124", "v125", "v126", "v127", "v128", "v129", "v130", "v131", "v132", "v133", "v134", "v135", "v136", "v137", "v138", "v139", "v148", "v149", "v150", "v151", "v152", "v153", "v154", "v155", "v156", "v157", "v158", "v159", "v160", "v161", "v162", "v163", "v164", "v165", "v166", "v167", "v168", "v169", "v170", "v171", "v172", "v173", "v174", "v175", "v176", "v177", "v178", "v179", "v180", "v181", "v182", "v183", "v184", "v185", "v186", "v187", "v188", "v189", "v190", "v191", "v192", "v193", "v194", "v195", "v196", "v197", "v198", "v199", "v200", "v201", "v202", "v203", "v204", "v205", "v206", "v207", "v208", "v209", "v210", "v211");
            lix = __builtin_amdgcn_rcpf(add1(blk ? lx[1] : lx[0], blk ? lx[3] : lx[2]));
            liy = __builtin_amdgcn_rcpf(add1(blk ? ly[1] : ly[0], blk ? ly[3] : ly[2]));
        } else {
            float mx = -__builtin_inff(), my = -__builtin_inff(), lx = 0.f, ly = 0.f;
            const f32x16 zero = {};
            ox = zero; oy = zero;
            auto step = [&](lptr c, const bf16x8& q0, const bf16x8& q1, float& m, float& l, f32x16& o) {
                f32x16 sc = MFMA32(ldsb128(c), q0, zero);
                sc = MFMA32(ldsb128(c + 1024), q1, sc);
                float t = sc[0];
#pragma unroll
                for (int i = 1; i < 16; ++i) t = fmaxf(t, sc[i]);
                const float mn = fmaxf(m, swap32max(t));
                const float al = __builtin_amdgcn_exp2f(m - mn);
                m = mn;
                float ps = 0.f;
#pragma unroll
                for (int i = 0; i < 16; ++i) { sc[i] = __builtin_amdgcn_exp2f(sc[i] - mn); ps += sc[i]; o[i] *= al; }
                l = l * al + ps;
                o = MFMA32(ldsb128(c + 2048), pack8(sc, 0), o);
                o = MFMA32(ldsb128(c + 3072), pack8(sc, 8), o);
            };
#pragma unroll 1
            for (int s = 0; s < 16; ++s) {
                lds_wait<1>(lds + M_FULL, (u32)(s + 1));
#pragma unroll 1
                for (int t = 0; t < 4; ++t) {
                    const lptr c = lds + L_KV + (s & 3) * 16384 + t * 4096 + vl16;
                    step(c, qx0, qx1, mx, lx, ox);
                    step(c, qy0, qy1, my, ly, oy);
                }
                asm volatile("s_waitcnt lgkmcnt(0)" ::: "memory");
                *(volatile LAS u32*)(lds + M_FREE + wid * 256 + lane * 4) = (u32)(s + 1);
            }
            lix = 1.f / swap32sum(lx); liy = 1.f / swap32sum(ly);
        }
        __builtin_amdgcn_s_setprio(3);
        lix *= 1.f - *(const LAS float*)(lds + M_G + (32 * par + r32) * 4);
        liy *= 1.f - *(const LAS float*)(lds + M_G + (32 * (1 - par) + r32) * 4);
        const u32 wa = ldsb + wid * 4096 + (16 * h + 4 * blk + q4) * 64 + blk * 32 + pp * 8;
        u32x16 wr0, wr1, wr2, wr3, wr4;
        u32x2 of = {0u, 0u};
        auto issue_w = [&](int j, u32 slot, int w) {
            const char* src = (const char*)p.wo + hg * 256 + w * 64 + (size_t)((rot3 + j) & 15) * 64 * 2048;
#pragma unroll
            for (int i = 0; i < 4; ++i) dma(voP, src + i * 16 * 2048, ldsb + slot + w * 4096 + i * 1024);
        };
        {
            const char* og = p.ws + WS_O + (size_t)(rt * 32 + hg * 4 + wid) * 4096;
            st16_dev(vl16, og + par * 2048, pack8s(ox, 0, lix));
            st16_dev(vl16, og + par * 2048 + 1024, pack8s(ox, 8, lix));
            st16_dev(vl16, og + (1 - par) * 2048, pack8s(oy, 0, liy));
            st16_dev(vl16, og + (1 - par) * 2048 + 1024, pack8s(oy, 8, liy));
            *(volatile LAS u32*)(lds + M_FREE + wid * 256) = 17u;
            if (wid == 0) ld8_issue(of, (lane & 7) * 8, p.ws + WS_OF + rt * 64);
            asm volatile("s_waitcnt vmcnt(0)" : "+v"(of)::"memory");
            u32 arrived;
            asm volatile("s_mov_b64 exec, 1\n\tds_add_rtn_u32 %0, %1, %2\n\ts_mov_b64 exec, -1\n\ts_waitcnt lgkmcnt(0)" : "=&v"(arrived) : "v"(ldsb + M_OJ), "v"(1u) : "memory");
            if ((rfl(arrived) & 15u) == 3u && lane == 0) st8g_dev(p.ws + WS_OF + (rt * 8 + hg) * 8, tag);
            lds_wait<1>(lds + M_WRDY, 1u);
    asm volatile(
        "ds_read_b64_tr_b16 v[20:21], %[w0h] offset:0\n\t"
        "ds_read_b64_tr_b16 v[22:23], %[w0h] offset:512\n\t"
        "ds_read_b64_tr_b16 v[24:25], %[w1h] offset:0\n\t"
        "ds_read_b64_tr_b16 v[26:27], %[w1h] offset:512\n\t"
        "ds_read_b64_tr_b16 v[28:29], %[w0h] offset:2048\n\t"
        "ds_read_b64_tr_b16 v[30:31], %[w0h] offset:2560\n\t"
        "ds_read_b64_tr_b16 v[32:33], %[w1h] offset:2048\n\t"
        "ds_read_b64_tr_b16 v[34:35], %[w1h] offset:2560\n\t"
        "ds_read_b64_tr_b16 v[36:37], %[w0h] offset:16384\n\t"
        "ds_read_b64_tr_b16 v[38:39], %[w0h] offset:16896\n\t"
        "ds_read_b64_tr_b16 v[40:41], %[w1h] offset:16384\n\t"
        "ds_read_b64_tr_b16 v[42:43], %[w1h] offset:16896\n\t"
        "ds_read_b64_tr_b16 v[44:45], %[w0h] offset:18432\n\t"
        "ds_read_b64_tr_b16 v[46:47], %[w0h] offset:18944\n\t"
        "ds_read_b64_tr_b16 v[48:49], %[w1h] offset:18432\n\t"
        "ds_read_b64_tr_b16 v[50:51], %[w1h] offset:18944\n\t"
        "ds_read_b64_tr_b16 v[52:53], %[w0] offset:0\n\t"
        "ds_read_b64_tr_b16 v[54:55], %[w0] offset:512\n\t"
        "ds_read_b64_tr_b16 v[56:57], %[w1] offset:0\n\t"
        "ds_read_b64_tr_b16 v[58:59], %[w1] offset:512\n\t"
        "ds_read_b64_tr_b16 v[60:61], %[w0] offset:2048\n\t"
        "ds_read_b64_tr_b16 v[62:63], %[w0] offset:2560\n\t"
        "ds_read_b64_tr_b16 v[64:65], %[w1] offset:2048\n\t"
        "ds_read_b64_tr_b16 v[66:67], %[w1] offset:2560\n\t"
        "ds_read_b64_tr_b16 v[68:69], %[w0] offset:16384\n\t"
        "ds_read_b64_tr_b16 v[70:71], %[w0] offset:16896\n\t"
        "ds_read_b64_tr_b16 v[72:73], %[w1] offset:16384\n\t"
        "ds_read_b64_tr_b16 v[74:75], %[w1] offset:16896\n\t"
        "ds_read_b64_tr_b16 v[76:77], %[w0] offset:18432\n\t"
        "ds_read_b64_tr_b16 v[78:79], %[w0] offset:18944\n\t"
        "ds_read_b64_tr_b16 v[80:81], %[w1] offset:18432\n\t"
        "ds_read_b64_tr_b16 v[82:83], %[w1] offset:18944\n\t"
        "ds_read_b64_tr_b16 v[84:85], %[w0] offset:32768\n\t"
        "ds_read_b64_tr_b16 v[86:87], %[w0] offset:33280\n\t"
        "ds_read_b64_tr_b16 v[88:89], %[w1] offset:32768\n\t"
        "ds_read_b64_tr_b16 v[90:91], %[w1] offset:33280\n\t"
        "ds_read_b64_tr_b16 v[92:93], %[w0] offset:34816\n\t"
        "ds_read_b64_tr_b16 v[94:95], %[w0] offset:35328\n\t"
        "ds_read_b64_tr_b16 v[96:97], %[w1] offset:34816\n\t"
        "ds_read_b64_tr_b16 v[98:99], %[w1] offset:35328\n\t"
        "s_waitcnt lgkmcnt(0)\n\t"
        : "=&{v[20:35]}"(wr0), "=&{v[36:51]}"(wr1), "=&{v[52:67]}"(wr2), "=&{v[68:83]}"(wr3), "=&{v[84:99]}"(wr4) : [w0] "v"(wa), [w1] "v"(wa ^ 32), [w0h] "v"(wa + 65536), [w1h] "v"((wa ^ 32) + 65536) : "memory");
            if (lane == 0) __hip_atomic_fetch_add((LAS u32*)(lds + M_OJ), 16u, __ATOMIC_RELAXED, __HIP_MEMORY_SCOPE_WORKGROUP);
            __builtin_amdgcn_s_setprio(0);
        }
        if (wid == 0) {
#pragma unroll 1
            for (u32 spins = 0; spins < SPIN_CAP; ++spins) {
                if (__all((lane & 7) == hg || (of[0] == (u32)tag && of[1] == (u32)(tag >> 32)))) break;
                ld8_issue(of, (lane & 7) * 8, p.ws + WS_OF + rt * 64);
                asm volatile("s_waitcnt vmcnt(0)" : "+v"(of)::"memory");
            }
            lds_wait<1>(lds + M_OJ, 68u);
            *(volatile LAS u32*)(lds + M_GO) = 1u;
            asm volatile("" ::: "memory");
        } else {
            lds_wait<1>(lds + M_OJ, 68u);
            issue_w(0, 65536, wid);
            if (wid == 1) issue_w(0, 65536, 0);
            issue_w(2, 81920, wid);
            if (wid == 2) issue_w(2, 81920, 0);
            issue_w(3, 0, wid); issue_w(5, 16384, wid);
            if (wid == 3) { issue_w(3, 0, 0); issue_w(5, 16384, 0); }
            lds_wait<1>(lds + M_GO, 1u);
        }
#pragma unroll
        for (int j = 0; j < 2; ++j)
#pragma unroll
            for (int i = 0; i < 4; ++i)
                dma_dev(vl16, p.ws + WS_O + (size_t)rt * 131072 + ((rot3 + 2 * j + (wid >> 1)) & 15) * 8192 + (4 * (wid & 1) + i) * 1024,
                        ldsb + 32768 + j * 16384 + wid * 4096 + i * 1024);
        if (lane == 0) __hip_atomic_fetch_add((LAS u32*)(lds + M_CR), 1u, __ATOMIC_RELAXED, __HIP_MEMORY_SCOPE_WORKGROUP);
        __builtin_amdgcn_s_setprio(2);
        f32x16 b0, b1;
        u32x16 zz, xx;
        const int c15 = lane & 15, g4 = lane >> 4;
        const u32 oa = ldsb + h * 1024 + blk * 512 + c15 * 16;
        u32 za[4], xa[2];
#pragma unroll
        for (int ro = 0; ro < 4; ++ro) za[ro] = ldsb + L_Z + (32 * wid + 4 * g4 + q4) * 128 + 16 * ((2 * ro + (pp >> 1)) ^ ((2 * blk + (q4 >> 1)) << 1)) + 8 * (pp & 1);
#pragma unroll
        for (int dl = 0; dl < 2; ++dl) xa[dl] = ldsb + L_X + c15 * 256 + (((4 * wid + 2 * dl + h) ^ c15) * 16) + 8 * blk;
    asm volatile(
        "s_waitcnt vmcnt(4) lgkmcnt(0)\n\t"
        "s_barrier\n\t"
        "ds_read_b64_tr_b16 v[196:197], %[w0h] offset:0\n\t"
        "ds_read_b64_tr_b16 v[198:199], %[w0h] offset:512\n\t"
        "ds_read_b64_tr_b16 v[200:201], %[w1h] offset:0\n\t"
        "ds_read_b64_tr_b16 v[202:203], %[w1h] offset:512\n\t"
        "ds_read_b128 v[132:135], %[x] offset:32768\n\t"
        "ds_read_b128 v[136:139], %[x] offset:33024\n\t"
        "ds_read_b128 v[140:143], %[x] offset:34816\n\t"
        "ds_read_b128 v[144:147], %[x] offset:35072\n\t"
        "ds_read_b64_tr_b16 v[204:205], %[w0h] offset:2048\n\t"
        "ds_read_b64_tr_b16 v[206:207], %[w0h] offset:2560\n\t"
        "ds_read_b64_tr_b16 v[208:209], %[w1h] offset:2048\n\t"
        "ds_read_b64_tr_b16 v[210:211], %[w1h] offset:2560\n\t"
        "ds_read_b128 v[148:151], %[x] offset:36864\n\t"
        "ds_read_b128 v[152:155], %[x] offset:37120\n\t"
        "ds_read_b128 v[156:159], %[x] offset:38912\n\t"
        "ds_read_b128 v[160:163], %[x] offset:39168\n\t"
        "s_waitcnt lgkmcnt(10)\n\t"
        "v_mfma_f32_16x16x32_bf16 v[100:103], v[196:199], v[132:135], 0\n\t"
        "v_mfma_f32_16x16x32_bf16 v[116:119], v[200:203], v[132:135], 0\n\t"
        "v_mfma_f32_16x16x32_bf16 v[104:107], v[196:199], v[136:139], 0\n\t"
        "v_mfma_f32_16x16x32_bf16 v[120:123], v[200:203], v[136:139], 0\n\t"
        "s_waitcnt lgkmcnt(0)\n\t"
        "s_barrier\n\t"
        "v_mfma_f32_16x16x32_bf16 v[108:111], v[196:199], v[140:143], 0\n\t"
        "ds_read_b128 v[164:167], %[x] offset:40960\n\t"
        "v_mfma_f32_16x16x32_bf16 v[124:127], v[200:203], v[140:143], 0\n\t"
        "ds_read_b128 v[168:171], %[x] offset:41216\n\t"
        "v_mfma_f32_16x16x32_bf16 v[112:115], v[196:199], v[144:147], 0\n\t"
        "v_mfma_f32_16x16x32_bf16 v[128:131], v[200:203], v[144:147], 0\n\t"
        "ds_read_b128 v[172:175], %[x] offset:43008\n\t"
        "v_mfma_f32_16x16x32_bf16 v[100:103], v[204:207], v[148:151], v[100:103]\n\t"
        "ds_read_b128 v[176:179], %[x] offset:43264\n\t"
        "v_mfma_f32_16x16x32_bf16 v[116:119], v[208:211], v[148:151], v[116:119]\n\t"
        "v_mfma_f32_16x16x32_bf16 v[104:107], v[204:207], v[152:155], v[104:107]\n\t"
        "ds_read_b128 v[180:183], %[x] offset:45056\n\t"
        "v_mfma_f32_16x16x32_bf16 v[120:123], v[208:211], v[152:155], v[120:123]\n\t"
        "ds_read_b128 v[184:187], %[x] offset:45312\n\t"
        "v_mfma_f32_16x16x32_bf16 v[108:111], v[204:207], v[156:159], v[108:111]\n\t"
        "v_mfma_f32_16x16x32_bf16 v[124:127], v[208:211], v[156:159], v[124:127]\n\t"
        "ds_read_b128 v[188:191], %[x] offset:47104\n\t"
        "v_mfma_f32_16x16x32_bf16 v[112:115], v[204:207], v[160:163], v[112:115]\n\t"
        "ds_read_b128 v[192:195], %[x] offset:47360\n\t"
        "v_mfma_f32_16x16x32_bf16 v[128:131], v[208:211], v[160:163], v[128:131]\n\t"
        "s_waitcnt lgkmcnt(6)\n\t"
        "v_mfma_f32_16x16x32_bf16 v[100:103], v[20:23], v[164:167], v[100:103]\n\t"
        "v_mfma_f32_16x16x32_bf16 v[116:119], v[24:27], v[164:167], v[116:119]\n\t"
        "v_mfma_f32_16x16x32_bf16 v[104:107], v[20:23], v[168:171], v[104:107]\n\t"
        "v_mfma_f32_16x16x32_bf16 v[120:123], v[24:27], v[168:171], v[120:123]\n\t"
        "s_waitcnt vmcnt(0) lgkmcnt(0)\n\t"
        "s_barrier\n\t"
        "v_mfma_f32_16x16x32_bf16 v[108:111], v[20:23], v[172:175], v[108:111]\n\t"
        "ds_read_b64_tr_b16 v[212:213], %[w0h] offset:16384\n\t"
        "ds_read_b64_tr_b16 v[214:215], %[w0h] offset:16896\n\t"
        "v_mfma_f32_16x16x32_bf16 v[124:127], v[24:27], v[172:175], v[124:127]\n\t"
        "ds_read_b64_tr_b16 v[216:217], %[w1h] offset:16384\n\t"
        "v_mfma_f32_16x16x32_bf16 v[112:115], v[20:23], v[176:179], v[112:115]\n\t"
        "ds_read_b64_tr_b16 v[218:219], %[w1h] offset:16896\n\t"
        "v_mfma_f32_16x16x32_bf16 v[128:131], v[24:27], v[176:179], v[128:131]\n\t"
        "ds_read_b128 v[132:135], %[x] offset:49152\n\t"
        "ds_read_b128 v[136:139], %[x] offset:49408\n\t"
        "v_mfma_f32_16x16x32_bf16 v[100:103], v[28:31], v[180:183], v[100:103]\n\t"
        "ds_read_b128 v[140:143], %[x] offset:51200\n\t"
        "v_mfma_f32_16x16x32_bf16 v[116:119], v[32:35], v[180:183], v[116:119]\n\t"
        "ds_read_b128 v[144:147], %[x] offset:51456\n\t"
        "v_mfma_f32_16x16x32_bf16 v[104:107], v[28:31], v[184:187], v[104:107]\n\t"
        "ds_read_b64_tr_b16 v[220:221], %[w0h] offset:18432\n\t"
        "ds_read_b64_tr_b16 v[222:223], %[w0h] offset:18944\n\t"
        "v_mfma_f32_16x16x32_bf16 v[120:123], v[32:35], v[184:187], v[120:123]\n\t"
        "ds_read_b64_tr_b16 v[224:225], %[w1h] offset:18432\n\t"
        "v_mfma_f32_16x16x32_bf16 v[108:111], v[28:31], v[188:191], v[108:111]\n\t"
        "ds_read_b64_tr_b16 v[226:227], %[w1h] offset:18944\n\t"
        "v_mfma_f32_16x16x32_bf16 v[124:127], v[32:35], v[188:191], v[124:127]\n\t"
        "ds_read_b128 v[148:151], %[x] offset:53248\n\t"
        "ds_read_b128 v[152:155], %[x] offset:53504\n\t"
        "v_mfma_f32_16x16x32_bf16 v[112:115], v[28:31], v[192:195], v[112:115]\n\t"
        "ds_read_b128 v[156:159], %[x] offset:55296\n\t"
        "v_mfma_f32_16x16x32_bf16 v[128:131], v[32:35], v[192:195], v[128:131]\n\t"
        "ds_read_b128 v[160:163], %[x] offset:55552\n\t"
        "s_waitcnt lgkmcnt(10)\n\t"
        "v_mfma_f32_16x16x32_bf16 v[100:103], v[212:215], v[132:135], v[100:103]\n\t"
        "v_mfma_f32_16x16x32_bf16 v[116:119], v[216:219], v[132:135], v[116:119]\n\t"
        "v_mfma_f32_16x16x32_bf16 v[104:107], v[212:215], v[136:139], v[104:107]\n\t"
        "v_mfma_f32_16x16x32_bf16 v[120:123], v[216:219], v[136:139], v[120:123]\n\t"
        "s_waitcnt lgkmcnt(0)\n\t"
        "s_barrier\n\t"
        "v_mfma_f32_16x16x32_bf16 v[108:111], v[212:215], v[140:143], v[108:111]\n\t"
        "ds_read_b64_tr_b16 v[196:197], %[w0] offset:0\n\t"
        "ds_read_b64_tr_b16 v[198:199], %[w0] offset:512\n\t"
        "v_mfma_f32_16x16x32_bf16 v[124:127], v[216:219], v[140:143], v[124:127]\n\t"
        "ds_read_b64_tr_b16 v[200:201], %[w1] offset:0\n\t"
        "v_mfma_f32_16x16x32_bf16 v[112:115], v[212:215], v[144:147], v[112:115]\n\t"
        "ds_read_b64_tr_b16 v[202:203], %[w1] offset:512\n\t"
        "v_mfma_f32_16x16x32_bf16 v[128:131], v[216:219], v[144:147], v[128:131]\n\t"
        "ds_read_b128 v[164:167], %[x] offset:57344\n\t"
        "ds_read_b128 v[168:171], %[x] offset:57600\n\t"
        "v_mfma_f32_16x16x32_bf16 v[100:103], v[220:223], v[148:151], v[100:103]\n\t"
        "ds_read_b128 v[172:175], %[x] offset:59392\n\t"
        "v_mfma_f32_16x16x32_bf16 v[116:119], v[224:227], v[148:151], v[116:119]\n\t"
        "ds_read_b128 v[176:179], %[x] offset:59648\n\t"
        "v_mfma_f32_16x16x32_bf16 v[104:107], v[220:223], v[152:155], v[104:107]\n\t"
        "ds_read_b64_tr_b16 v[204:205], %[w0] offset:2048\n\t"
        "ds_read_b64_tr_b16 v[206:207], %[w0] offset:2560\n\t"
        "v_mfma_f32_16x16x32_bf16 v[120:123], v[224:227], v[152:155], v[120:123]\n\t"
        "ds_read_b64_tr_b16 v[208:209], %[w1] offset:2048\n\t"
        "v_mfma_f32_16x16x32_bf16 v[108:111], v[220:223], v[156:159], v[108:111]\n\t"
        "ds_read_b64_tr_b16 v[210:211], %[w1] offset:2560\n\t"
        "v_mfma_f32_16x16x32_bf16 v[124:127], v[224:227], v[156:159], v[124:127]\n\t"
        "ds_read_b128 v[180:183], %[x] offset:61440\n\t"
        "ds_read_b128 v[184:187], %[x] offset:61696\n\t"
        "v_mfma_f32_16x16x32_bf16 v[112:115], v[220:223], v[160:163], v[112:115]\n\t"
        "ds_read_b128 v[188:191], %[x] offset:63488\n\t"
        "v_mfma_f32_16x16x32_bf16 v[128:131], v[224:227], v[160:163], v[128:131]\n\t"
        "ds_read_b128 v[192:195], %[x] offset:63744\n\t"
        "s_waitcnt lgkmcnt(10)\n\t"
        "v_mfma_f32_16x16x32_bf16 v[100:103], v[196:199], v[164:167], v[100:103]\n\t"
        "v_mfma_f32_16x16x32_bf16 v[116:119], v[200:203], v[164:167], v[116:119]\n\t"
        "v_mfma_f32_16x16x32_bf16 v[104:107], v[196:199], v[168:171], v[104:107]\n\t"
        "v_mfma_f32_16x16x32_bf16 v[120:123], v[200:203], v[168:171], v[120:123]\n\t"
        "s_waitcnt lgkmcnt(0)\n\t"
        "s_barrier\n\t"
        "v_mfma_f32_16x16x32_bf16 v[108:111], v[196:199], v[172:175], v[108:111]\n\t"
        "ds_read_b128 v[132:135], %[xh] offset:0\n\t"
        "v_mfma_f32_16x16x32_bf16 v[124:127], v[200:203], v[172:175], v[124:127]\n\t"
        "ds_read_b128 v[136:139], %[xh] offset:256\n\t"
        "v_mfma_f32_16x16x32_bf16 v[112:115], v[196:199], v[176:179], v[112:115]\n\t"
        "v_mfma_f32_16x16x32_bf16 v[128:131], v[200:203], v[176:179], v[128:131]\n\t"
        "ds_read_b128 v[140:143], %[xh] offset:2048\n\t"
        "v_mfma_f32_16x16x32_bf16 v[100:103], v[204:207], v[180:183], v[100:103]\n\t"
        "ds_read_b128 v[144:147], %[xh] offset:2304\n\t"
        "v_mfma_f32_16x16x32_bf16 v[116:119], v[208:211], v[180:183], v[116:119]\n\t"
        "v_mfma_f32_16x16x32_bf16 v[104:107], v[204:207], v[184:187], v[104:107]\n\t"
        "ds_read_b128 v[148:151], %[xh] offset:4096\n\t"
        "v_mfma_f32_16x16x32_bf16 v[120:123], v[208:211], v[184:187], v[120:123]\n\t"
        "ds_read_b128 v[152:155], %[xh] offset:4352\n\t"
        "v_mfma_f32_16x16x32_bf16 v[108:111], v[204:207], v[188:191], v[108:111]\n\t"
        "v_mfma_f32_16x16x32_bf16 v[124:127], v[208:211], v[188:191], v[124:127]\n\t"
        "ds_read_b128 v[156:159], %[xh] offset:6144\n\t"
        "v_mfma_f32_16x16x32_bf16 v[112:115], v[204:207], v[192:195], v[112:115]\n\t"
        "ds_read_b128 v[160:163], %[xh] offset:6400\n\t"
        "v_mfma_f32_16x16x32_bf16 v[128:131], v[208:211], v[192:195], v[128:131]\n\t"
        "s_waitcnt lgkmcnt(6)\n\t"
        "v_mfma_f32_16x16x32_bf16 v[100:103], v[36:39], v[132:135], v[100:103]\n\t"
        "v_mfma_f32_16x16x32_bf16 v[116:119], v[40:43], v[132:135], v[116:119]\n\t"
        "v_mfma_f32_16x16x32_bf16 v[104:107], v[36:39], v[136:139], v[104:107]\n\t"
        "v_mfma_f32_16x16x32_bf16 v[120:123], v[40:43], v[136:139], v[120:123]\n\t"
        "s_waitcnt lgkmcnt(0)\n\t"
        "s_barrier\n\t"
        "v_mfma_f32_16x16x32_bf16 v[108:111], v[36:39], v[140:143], v[108:111]\n\t"
        "ds_read_b64_tr_b16 v[212:213], %[w0] offset:16384\n\t"
        "ds_read_b64_tr_b16 v[214:215], %[w0] offset:16896\n\t"
        "v_mfma_f32_16x16x32_bf16 v[124:127], v[40:43], v[140:143], v[124:127]\n\t"
        "ds_read_b64_tr_b16 v[216:217], %[w1] offset:16384\n\t"
        "v_mfma_f32_16x16x32_bf16 v[112:115], v[36:39], v[144:147], v[112:115]\n\t"
        "ds_read_b64_tr_b16 v[218:219], %[w1] offset:16896\n\t"
        "v_mfma_f32_16x16x32_bf16 v[128:131], v[40:43], v[144:147], v[128:131]\n\t"
        "ds_read_b128 v[164:167], %[xh] offset:8192\n\t"
        "ds_read_b128 v[168:171], %[xh] offset:8448\n\t"
        "v_mfma_f32_16x16x32_bf16 v[100:103], v[44:47], v[148:151], v[100:103]\n\t"
        "ds_read_b128 v[172:175], %[xh] offset:10240\n\t"
        "v_mfma_f32_16x16x32_bf16 v[116:119], v[48:51], v[148:151], v[116:119]\n\t"
        "ds_read_b128 v[176:179], %[xh] offset:10496\n\t"
        "v_mfma_f32_16x16x32_bf16 v[104:107], v[44:47], v[152:155], v[104:107]\n\t"
        "ds_read_b64_tr_b16 v[220:221], %[w0] offset:18432\n\t"
        "ds_read_b64_tr_b16 v[222:223], %[w0] offset:18944\n\t"
        "v_mfma_f32_16x16x32_bf16 v[120:123], v[48:51], v[152:155], v[120:123]\n\t"
        "ds_read_b64_tr_b16 v[224:225], %[w1] offset:18432\n\t"
        "v_mfma_f32_16x16x32_bf16 v[108:111], v[44:47], v[156:159], v[108:111]\n\t"
        "ds_read_b64_tr_b16 v[226:227], %[w1] offset:18944\n\t"
        "v_mfma_f32_16x16x32_bf16 v[124:127], v[48:51], v[156:159], v[124:127]\n\t"
        "ds_read_b128 v[180:183], %[xh] offset:12288\n\t"
        "ds_read_b128 v[184:187], %[xh] offset:12544\n\t"
        "v_mfma_f32_16x16x32_bf16 v[112:115], v[44:47], v[160:163], v[112:115]\n\t"
        "ds_read_b128 v[188:191], %[xh] offset:14336\n\t"
        "v_mfma_f32_16x16x32_bf16 v[128:131], v[48:51], v[160:163], v[128:131]\n\t"
        "ds_read_b128 v[192:195], %[xh] offset:14592\n\t"
        "s_waitcnt lgkmcnt(10)\n\t"
        "v_mfma_f32_16x16x32_bf16 v[100:103], v[212:215], v[164:167], v[100:103]\n\t"
        "v_mfma_f32_16x16x32_bf16 v[116:119], v[216:219], v[164:167], v[116:119]\n\t"
        "v_mfma_f32_16x16x32_bf16 v[104:107], v[212:215], v[168:171], v[104:107]\n\t"
        "v_mfma_f32_16x16x32_bf16 v[120:123], v[216:219], v[168:171], v[120:123]\n\t"
        "s_waitcnt lgkmcnt(0)\n\t"
        "s_barrier\n\t"
        "v_mfma_f32_16x16x32_bf16 v[108:111], v[212:215], v[172:175], v[108:111]\n\t"
        "ds_read_b64_tr_b16 v[196:197], %[w0h] offset:0\n\t"
        "ds_read_b64_tr_b16 v[198:199], %[w0h] offset:512\n\t"
        "v_mfma_f32_16x16x32_bf16 v[124:127], v[216:219], v[172:175], v[124:127]\n\t"
        "ds_read_b64_tr_b16 v[200:201], %[w1h] offset:0\n\t"
        "v_mfma_f32_16x16x32_bf16 v[112:115], v[212:215], v[176:179], v[112:115]\n\t"
        "ds_read_b64_tr_b16 v[202:203], %[w1h] offset:512\n\t"
        "v_mfma_f32_16x16x32_bf16 v[128:131], v[216:219], v[176:179], v[128:131]\n\t"
        "ds_read_b128 v[132:135], %[xh] offset:16384\n\t"
        "ds_read_b128 v[136:139], %[xh] offset:16640\n\t"
        "v_mfma_f32_16x16x32_bf16 v[100:103], v[220:223], v[180:183], v[100:103]\n\t"
        "ds_read_b128 v[140:143], %[xh] offset:18432\n\t"
        "v_mfma_f32_16x16x32_bf16 v[116:119], v[224:227], v[180:183], v[116:119]\n\t"
        "ds_read_b128 v[144:147], %[xh] offset:18688\n\t"
        "v_mfma_f32_16x16x32_bf16 v[104:107], v[220:223], v[184:187], v[104:107]\n\t"
        "ds_read_b64_tr_b16 v[204:205], %[w0h] offset:2048\n\t"
        "ds_read_b64_tr_b16 v[206:207], %[w0h] offset:2560\n\t"
        "v_mfma_f32_16x16x32_bf16 v[120:123], v[224:227], v[184:187], v[120:123]\n\t"
        "ds_read_b64_tr_b16 v[208:209], %[w1h] offset:2048\n\t"
        "v_mfma_f32_16x16x32_bf16 v[108:111], v[220:223], v[188:191], v[108:111]\n\t"
        "ds_read_b64_tr_b16 v[210:211], %[w1h] offset:2560\n\t"
        "v_mfma_f32_16x16x32_bf16 v[124:127], v[224:227], v[188:191], v[124:127]\n\t"
        "ds_read_b128 v[148:151], %[xh] offset:20480\n\t"
        "ds_read_b128 v[152:155], %[xh] offset:20736\n\t"
        "v_mfma_f32_16x16x32_bf16 v[112:115], v[220:223], v[192:195], v[112:115]\n\t"
        "ds_read_b128 v[156:159], %[xh] offset:22528\n\t"
        "v_mfma_f32_16x16x32_bf16 v[128:131], v[224:227], v[192:195], v[128:131]\n\t"
        "ds_read_b128 v[160:163], %[xh] offset:22784\n\t"
        "s_waitcnt lgkmcnt(10)\n\t"
        "v_mfma_f32_16x16x32_bf16 v[100:103], v[196:199], v[132:135], v[100:103]\n\t"
        "v_mfma_f32_16x16x32_bf16 v[116:119], v[200:203], v[132:135], v[116:119]\n\t"
        "v_mfma_f32_16x16x32_bf16 v[104:107], v[196:199], v[136:139], v[104:107]\n\t"
        "v_mfma_f32_16x16x32_bf16 v[120:123], v[200:203], v[136:139], v[120:123]\n\t"
        "s_waitcnt lgkmcnt(0)\n\t"
        "s_barrier\n\t"
        "v_mfma_f32_16x16x32_bf16 v[108:111], v[196:199], v[140:143], v[108:111]\n\t"
        "ds_read_b128 v[164:167], %[xh] offset:24576\n\t"
        "v_mfma_f32_16x16x32_bf16 v[124:127], v[200:203], v[140:143], v[124:127]\n\t"
        "ds_read_b128 v[168:171], %[xh] offset:24832\n\t"
        "v_mfma_f32_16x16x32_bf16 v[112:115], v[196:199], v[144:147], v[112:115]\n\t"
        "v_mfma_f32_16x16x32_bf16 v[128:131], v[200:203], v[144:147], v[128:131]\n\t"
        "ds_read_b128 v[172:175], %[xh] offset:26624\n\t"
        "v_mfma_f32_16x16x32_bf16 v[100:103], v[204:207], v[148:151], v[100:103]\n\t"
        "ds_read_b128 v[176:179], %[xh] offset:26880\n\t"
        "v_mfma_f32_16x16x32_bf16 v[116:119], v[208:211], v[148:151], v[116:119]\n\t"
        "v_mfma_f32_16x16x32_bf16 v[104:107], v[204:207], v[152:155], v[104:107]\n\t"
        "ds_read_b128 v[180:183], %[xh] offset:28672\n\t"
        "v_mfma_f32_16x16x32_bf16 v[120:123], v[208:211], v[152:155], v[120:123]\n\t"
        "ds_read_b128 v[184:187], %[xh] offset:28928\n\t"
        "v_mfma_f32_16x16x32_bf16 v[108:111], v[204:207], v[156:159], v[108:111]\n\t"
        "v_mfma_f32_16x16x32_bf16 v[124:127], v[208:211], v[156:159], v[124:127]\n\t"
        "ds_read_b128 v[188:191], %[xh] offset:30720\n\t"
        "v_mfma_f32_16x16x32_bf16 v[112:115], v[204:207], v[160:163], v[112:115]\n\t"
        "ds_read_b128 v[192:195], %[xh] offset:30976\n\t"
        "v_mfma_f32_16x16x32_bf16 v[128:131], v[208:211], v[160:163], v[128:131]\n\t"
        "s_waitcnt lgkmcnt(6)\n\t"
        "v_mfma_f32_16x16x32_bf16 v[100:103], v[52:55], v[164:167], v[100:103]\n\t"
        "v_mfma_f32_16x16x32_bf16 v[116:119], v[56:59], v[164:167], v[116:119]\n\t"
        "v_mfma_f32_16x16x32_bf16 v[104:107], v[52:55], v[168:171], v[104:107]\n\t"
        "v_mfma_f32_16x16x32_bf16 v[120:123], v[56:59], v[168:171], v[120:123]\n\t"
        "s_waitcnt lgkmcnt(0)\n\t"
        "s_barrier\n\t"
        "v_mfma_f32_16x16x32_bf16 v[108:111], v[52:55], v[172:175], v[108:111]\n\t"
        "ds_read_b64_tr_b16 v[212:213], %[w0h] offset:16384\n\t"
        "ds_read_b64_tr_b16 v[214:215], %[w0h] offset:16896\n\t"
        "v_mfma_f32_16x16x32_bf16 v[124:127], v[56:59], v[172:175], v[124:127]\n\t"
        "ds_read_b64_tr_b16 v[216:217], %[w1h] offset:16384\n\t"
        "v_mfma_f32_16x16x32_bf16 v[112:115], v[52:55], v[176:179], v[112:115]\n\t"
        "ds_read_b64_tr_b16 v[218:219], %[w1h] offset:16896\n\t"
        "v_mfma_f32_16x16x32_bf16 v[128:131], v[56:59], v[176:179], v[128:131]\n\t"
        "ds_read_b128 v[132:135], %[x] offset:32768\n\t"
        "ds_read_b128 v[136:139], %[x] offset:33024\n\t"
        "v_mfma_f32_16x16x32_bf16 v[100:103], v[60:63], v[180:183], v[100:103]\n\t"
        "ds_read_b128 v[140:143], %[x] offset:34816\n\t"
        "v_mfma_f32_16x16x32_bf16 v[116:119], v[64:67], v[180:183], v[116:119]\n\t"
        "ds_read_b128 v[144:147], %[x] offset:35072\n\t"
        "v_mfma_f32_16x16x32_bf16 v[104:107], v[60:63], v[184:187], v[104:107]\n\t"
        "ds_read_b64_tr_b16 v[220:221], %[w0h] offset:18432\n\t"
        "ds_read_b64_tr_b16 v[222:223], %[w0h] offset:18944\n\t"
        "v_mfma_f32_16x16x32_bf16 v[120:123], v[64:67], v[184:187], v[120:123]\n\t"
        "ds_read_b64_tr_b16 v[224:225], %[w1h] offset:18432\n\t"
        "v_mfma_f32_16x16x32_bf16 v[108:111], v[60:63], v[188:191], v[108:111]\n\t"
        "ds_read_b64_tr_b16 v[226:227], %[w1h] offset:18944\n\t"
        "v_mfma_f32_16x16x32_bf16 v[124:127], v[64:67], v[188:191], v[124:127]\n\t"
        "ds_read_b128 v[148:151], %[x] offset:36864\n\t"
        "ds_read_b128 v[152:155], %[x] offset:37120\n\t"
        "v_mfma_f32_16x16x32_bf16 v[112:115], v[60:63], v[192:195], v[112:115]\n\t"
        "ds_read_b128 v[156:159], %[x] offset:38912\n\t"
        "v_mfma_f32_16x16x32_bf16 v[128:131], v[64:67], v[192:195], v[128:131]\n\t"
        "ds_read_b128 v[160:163], %[x] offset:39168\n\t"
        "s_waitcnt lgkmcnt(10)\n\t"
        "v_mfma_f32_16x16x32_bf16 v[100:103], v[212:215], v[132:135], v[100:103]\n\t"
        "v_mfma_f32_16x16x32_bf16 v[116:119], v[216:219], v[132:135], v[116:119]\n\t"
        "v_mfma_f32_16x16x32_bf16 v[104:107], v[212:215], v[136:139], v[104:107]\n\t"
        "v_mfma_f32_16x16x32_bf16 v[120:123], v[216:219], v[136:139], v[120:123]\n\t"
        "s_waitcnt lgkmcnt(0)\n\t"
        "s_barrier\n\t"
        "v_mfma_f32_16x16x32_bf16 v[108:111], v[212:215], v[140:143], v[108:111]\n\t"
        "ds_read_b64_tr_b16 v[196:197], %[w0] offset:0\n\t"
        "ds_read_b64_tr_b16 v[198:199], %[w0] offset:512\n\t"
        "v_mfma_f32_16x16x32_bf16 v[124:127], v[216:219], v[140:143], v[124:127]\n\t"
        "ds_read_b64_tr_b16 v[200:201], %[w1] offset:0\n\t"
        "v_mfma_f32_16x16x32_bf16 v[112:115], v[212:215], v[144:147], v[112:115]\n\t"
        "ds_read_b64_tr_b16 v[202:203], %[w1] offset:512\n\t"
        "v_mfma_f32_16x16x32_bf16 v[128:131], v[216:219], v[144:147], v[128:131]\n\t"
        "ds_read_b128 v[164:167], %[x] offset:40960\n\t"
        "ds_read_b128 v[168:171], %[x] offset:41216\n\t"
        "v_mfma_f32_16x16x32_bf16 v[100:103], v[220:223], v[148:151], v[100:103]\n\t"
        "ds_read_b128 v[172:175], %[x] offset:43008\n\t"
        "v_mfma_f32_16x16x32_bf16 v[116:119], v[224:227], v[148:151], v[116:119]\n\t"
        "ds_read_b128 v[176:179], %[x] offset:43264\n\t"
        "v_mfma_f32_16x16x32_bf16 v[104:107], v[220:223], v[152:155], v[104:107]\n\t"
        "ds_read_b64_tr_b16 v[204:205], %[w0] offset:2048\n\t"
        "ds_read_b64_tr_b16 v[206:207], %[w0] offset:2560\n\t"
        "v_mfma_f32_16x16x32_bf16 v[120:123], v[224:227], v[152:155], v[120:123]\n\t"
        "ds_read_b64_tr_b16 v[208:209], %[w1] offset:2048\n\t"
        "v_mfma_f32_16x16x32_bf16 v[108:111], v[220:223], v[156:159], v[108:111]\n\t"
        "ds_read_b64_tr_b16 v[210:211], %[w1] offset:2560\n\t"
        "v_mfma_f32_16x16x32_bf16 v[124:127], v[224:227], v[156:159], v[124:127]\n\t"
        "ds_read_b128 v[180:183], %[x] offset:45056\n\t"
        "ds_read_b128 v[184:187], %[x] offset:45312\n\t"
        "v_mfma_f32_16x16x32_bf16 v[112:115], v[220:223], v[160:163], v[112:115]\n\t"
        "ds_read_b128 v[188:191], %[x] offset:47104\n\t"
        "v_mfma_f32_16x16x32_bf16 v[128:131], v[224:227], v[160:163], v[128:131]\n\t"
        "ds_read_b128 v[192:195], %[x] offset:47360\n\t"
        "s_waitcnt lgkmcnt(10)\n\t"
        "v_mfma_f32_16x16x32_bf16 v[100:103], v[196:199], v[164:167], v[100:103]\n\t"
        "v_mfma_f32_16x16x32_bf16 v[116:119], v[200:203], v[164:167], v[116:119]\n\t"
        "v_mfma_f32_16x16x32_bf16 v[104:107], v[196:199], v[168:171], v[104:107]\n\t"
        "v_mfma_f32_16x16x32_bf16 v[120:123], v[200:203], v[168:171], v[120:123]\n\t"
        "s_waitcnt lgkmcnt(0)\n\t"
        "s_barrier\n\t"
        "v_mfma_f32_16x16x32_bf16 v[108:111], v[196:199], v[172:175], v[108:111]\n\t"
        "ds_read_b128 v[132:135], %[x] offset:49152\n\t"
        "v_mfma_f32_16x16x32_bf16 v[124:127], v[200:203], v[172:175], v[124:127]\n\t"
        "ds_read_b128 v[136:139], %[x] offset:49408\n\t"
        "v_mfma_f32_16x16x32_bf16 v[112:115], v[196:199], v[176:179], v[112:115]\n\t"
        "v_mfma_f32_16x16x32_bf16 v[128:131], v[200:203], v[176:179], v[128:131]\n\t"
        "ds_read_b128 v[140:143], %[x] offset:51200\n\t"
        "v_mfma_f32_16x16x32_bf16 v[100:103], v[204:207], v[180:183], v[100:103]\n\t"
        "ds_read_b128 v[144:147], %[x] offset:51456\n\t"
        "v_mfma_f32_16x16x32_bf16 v[116:119], v[208:211], v[180:183], v[116:119]\n\t"
        "v_mfma_f32_16x16x32_bf16 v[104:107], v[204:207], v[184:187], v[104:107]\n\t"
        "ds_read_b128 v[148:151], %[x] offset:53248\n\t"
        "v_mfma_f32_16x16x32_bf16 v[120:123], v[208:211], v[184:187], v[120:123]\n\t"
        "ds_read_b128 v[152:155], %[x] offset:53504\n\t"
        "v_mfma_f32_16x16x32_bf16 v[108:111], v[204:207], v[188:191], v[108:111]\n\t"
        "v_mfma_f32_16x16x32_bf16 v[124:127], v[208:211], v[188:191], v[124:127]\n\t"
        "ds_read_b128 v[156:159], %[x] offset:55296\n\t"
        "v_mfma_f32_16x16x32_bf16 v[112:115], v[204:207], v[192:195], v[112:115]\n\t"
        "ds_read_b128 v[160:163], %[x] offset:55552\n\t"
        "v_mfma_f32_16x16x32_bf16 v[128:131], v[208:211], v[192:195], v[128:131]\n\t"
        "s_waitcnt lgkmcnt(6)\n\t"
        "v_mfma_f32_16x16x32_bf16 v[100:103], v[68:71], v[132:135], v[100:103]\n\t"
        "v_mfma_f32_16x16x32_bf16 v[116:119], v[72:75], v[132:135], v[116:119]\n\t"
        "v_mfma_f32_16x16x32_bf16 v[104:107], v[68:71], v[136:139], v[104:107]\n\t"
        "v_mfma_f32_16x16x32_bf16 v[120:123], v[72:75], v[136:139], v[120:123]\n\t"
        "s_waitcnt lgkmcnt(0)\n\t"
        "s_barrier\n\t"
        "v_mfma_f32_16x16x32_bf16 v[108:111], v[68:71], v[140:143], v[108:111]\n\t"
        "ds_read_b64_tr_b16 v[212:213], %[w0] offset:16384\n\t"
        "ds_read_b64_tr_b16 v[214:215], %[w0] offset:16896\n\t"
        "v_mfma_f32_16x16x32_bf16 v[124:127], v[72:75], v[140:143], v[124:127]\n\t"
        "ds_read_b64_tr_b16 v[216:217], %[w1] offset:16384\n\t"
        "v_mfma_f32_16x16x32_bf16 v[112:115], v[68:71], v[144:147], v[112:115]\n\t"
        "ds_read_b64_tr_b16 v[218:219], %[w1] offset:16896\n\t"
        "v_mfma_f32_16x16x32_bf16 v[128:131], v[72:75], v[144:147], v[128:131]\n\t"
        "ds_read_b128 v[164:167], %[x] offset:57344\n\t"
        "ds_read_b128 v[168:171], %[x] offset:57600\n\t"
        "v_mfma_f32_16x16x32_bf16 v[100:103], v[76:79], v[148:151], v[100:103]\n\t"
        "ds_read_b128 v[172:175], %[x] offset:59392\n\t"
        "v_mfma_f32_16x16x32_bf16 v[116:119], v[80:83], v[148:151], v[116:119]\n\t"
        "ds_read_b128 v[176:179], %[x] offset:59648\n\t"
        "v_mfma_f32_16x16x32_bf16 v[104:107], v[76:79], v[152:155], v[104:107]\n\t"
        "ds_read_b64_tr_b16 v[220:221], %[w0] offset:18432\n\t"
        "ds_read_b64_tr_b16 v[222:223], %[w0] offset:18944\n\t"
        "v_mfma_f32_16x16x32_bf16 v[120:123], v[80:83], v[152:155], v[120:123]\n\t"
        "ds_read_b64_tr_b16 v[224:225], %[w1] offset:18432\n\t"
        "v_mfma_f32_16x16x32_bf16 v[108:111], v[76:79], v[156:159], v[108:111]\n\t"
        "ds_read_b64_tr_b16 v[226:227], %[w1] offset:18944\n\t"
        "v_mfma_f32_16x16x32_bf16 v[124:127], v[80:83], v[156:159], v[124:127]\n\t"
        "ds_read_b128 v[180:183], %[x] offset:61440\n\t"
        "ds_read_b128 v[184:187], %[x] offset:61696\n\t"
        "v_mfma_f32_16x16x32_bf16 v[112:115], v[76:79], v[160:163], v[112:115]\n\t"
        "ds_read_b128 v[188:191], %[x] offset:63488\n\t"
        "v_mfma_f32_16x16x32_bf16 v[128:131], v[80:83], v[160:163], v[128:131]\n\t"
        "ds_read_b128 v[192:195], %[x] offset:63744\n\t"
        "s_waitcnt lgkmcnt(10)\n\t"
        "v_mfma_f32_16x16x32_bf16 v[100:103], v[212:215], v[164:167], v[100:103]\n\t"
        "v_mfma_f32_16x16x32_bf16 v[116:119], v[216:219], v[164:167], v[116:119]\n\t"
        "v_mfma_f32_16x16x32_bf16 v[104:107], v[212:215], v[168:171], v[104:107]\n\t"
        "v_mfma_f32_16x16x32_bf16 v[120:123], v[216:219], v[168:171], v[120:123]\n\t"
        "s_waitcnt lgkmcnt(0)\n\t"
        "s_barrier\n\t"
        "v_mfma_f32_16x16x32_bf16 v[108:111], v[212:215], v[172:175], v[108:111]\n\t"
        "ds_read_b64_tr_b16 v[196:197], %[w0h] offset:0\n\t"
        "ds_read_b64_tr_b16 v[198:199], %[w0h] offset:512\n\t"
        "v_mfma_f32_16x16x32_bf16 v[124:127], v[216:219], v[172:175], v[124:127]\n\t"
        "ds_read_b64_tr_b16 v[200:201], %[w1h] offset:0\n\t"
        "v_mfma_f32_16x16x32_bf16 v[112:115], v[212:215], v[176:179], v[112:115]\n\t"
        "ds_read_b64_tr_b16 v[202:203], %[w1h] offset:512\n\t"
        "v_mfma_f32_16x16x32_bf16 v[128:131], v[216:219], v[176:179], v[128:131]\n\t"
        "ds_read_b128 v[132:135], %[xh] offset:0\n\t"
        "ds_read_b128 v[136:139], %[xh] offset:256\n\t"
        "v_mfma_f32_16x16x32_bf16 v[100:103], v[220:223], v[180:183], v[100:103]\n\t"
        "ds_read_b128 v[140:143], %[xh] offset:2048\n\t"
        "v_mfma_f32_16x16x32_bf16 v[116:119], v[224:227], v[180:183], v[116:119]\n\t"
        "ds_read_b128 v[144:147], %[xh] offset:2304\n\t"
        "v_mfma_f32_16x16x32_bf16 v[104:107], v[220:223], v[184:187], v[104:107]\n\t"
        "ds_read_b64_tr_b16 v[204:205], %[w0h] offset:2048\n\t"
        "ds_read_b64_tr_b16 v[206:207], %[w0h] offset:2560\n\t"
        "v_mfma_f32_16x16x32_bf16 v[120:123], v[224:227], v[184:187], v[120:123]\n\t"
        "ds_read_b64_tr_b16 v[208:209], %[w1h] offset:2048\n\t"
        "v_mfma_f32_16x16x32_bf16 v[108:111], v[220:223], v[188:191], v[108:111]\n\t"
        "ds_read_b64_tr_b16 v[210:211], %[w1h] offset:2560\n\t"
        "v_mfma_f32_16x16x32_bf16 v[124:127], v[224:227], v[188:191], v[124:127]\n\t"
        "ds_read_b128 v[148:151], %[xh] offset:4096\n\t"
        "ds_read_b128 v[152:155], %[xh] offset:4352\n\t"
        "v_mfma_f32_16x16x32_bf16 v[112:115], v[220:223], v[192:195], v[112:115]\n\t"
        "ds_read_b128 v[156:159], %[xh] offset:6144\n\t"
        "v_mfma_f32_16x16x32_bf16 v[128:131], v[224:227], v[192:195], v[128:131]\n\t"
        "ds_read_b128 v[160:163], %[xh] offset:6400\n\t"
        "s_waitcnt lgkmcnt(10)\n\t"
        "v_mfma_f32_16x16x32_bf16 v[100:103], v[196:199], v[132:135], v[100:103]\n\t"
        "v_mfma_f32_16x16x32_bf16 v[116:119], v[200:203], v[132:135], v[116:119]\n\t"
        "v_mfma_f32_16x16x32_bf16 v[104:107], v[196:199], v[136:139], v[104:107]\n\t"
        "v_mfma_f32_16x16x32_bf16 v[120:123], v[200:203], v[136:139], v[120:123]\n\t"
        "s_waitcnt lgkmcnt(0)\n\t"
        "s_barrier\n\t"
        "v_mfma_f32_16x16x32_bf16 v[108:111], v[196:199], v[140:143], v[108:111]\n\t"
        "ds_read_b128 v[164:167], %[xh] offset:8192\n\t"
        "v_mfma_f32_16x16x32_bf16 v[124:127], v[200:203], v[140:143], v[124:127]\n\t"
        "ds_read_b128 v[168:171], %[xh] offset:8448\n\t"
        "v_mfma_f32_16x16x32_bf16 v[112:115], v[196:199], v[144:147], v[112:115]\n\t"
        "v_mfma_f32_16x16x32_bf16 v[128:131], v[200:203], v[144:147], v[128:131]\n\t"
        "ds_read_b128 v[172:175], %[xh] offset:10240\n\t"
        "v_mfma_f32_16x16x32_bf16 v[100:103], v[204:207], v[148:151], v[100:103]\n\t"
        "ds_read_b128 v[176:179], %[xh] offset:10496\n\t"
        "v_mfma_f32_16x16x32_bf16 v[116:119], v[208:211], v[148:151], v[116:119]\n\t"
        "v_mfma_f32_16x16x32_bf16 v[104:107], v[204:207], v[152:155], v[104:107]\n\t"
        "ds_read_b128 v[180:183], %[xh] offset:12288\n\t"
        "v_mfma_f32_16x16x32_bf16 v[120:123], v[208:211], v[152:155], v[120:123]\n\t"
        "ds_read_b128 v[184:187], %[xh] offset:12544\n\t"
        "v_mfma_f32_16x16x32_bf16 v[108:111], v[204:207], v[156:159], v[108:111]\n\t"
        "v_mfma_f32_16x16x32_bf16 v[124:127], v[208:211], v[156:159], v[124:127]\n\t"
        "ds_read_b128 v[188:191], %[xh] offset:14336\n\t"
        "v_mfma_f32_16x16x32_bf16 v[112:115], v[204:207], v[160:163], v[112:115]\n\t"
        "ds_read_b128 v[192:195], %[xh] offset:14592\n\t"
        "v_mfma_f32_16x16x32_bf16 v[128:131], v[208:211], v[160:163], v[128:131]\n\t"
        "s_waitcnt lgkmcnt(6)\n\t"
        "v_mfma_f32_16x16x32_bf16 v[100:103], v[84:87], v[164:167], v[100:103]\n\t"
        "v_mfma_f32_16x16x32_bf16 v[116:119], v[88:91], v[164:167], v[116:119]\n\t"
        "v_mfma_f32_16x16x32_bf16 v[104:107], v[84:87], v[168:171], v[104:107]\n\t"
        "v_mfma_f32_16x16x32_bf16 v[120:123], v[88:91], v[168:171], v[120:123]\n\t"
        "s_waitcnt lgkmcnt(0)\n\t"
        "s_barrier\n\t"
        "v_mfma_f32_16x16x32_bf16 v[108:111], v[84:87], v[172:175], v[108:111]\n\t"
        "ds_read_b64_tr_b16 v[212:213], %[w0h] offset:16384\n\t"
        "ds_read_b64_tr_b16 v[214:215], %[w0h] offset:16896\n\t"
        "v_mfma_f32_16x16x32_bf16 v[124:127], v[88:91], v[172:175], v[124:127]\n\t"
        "ds_read_b64_tr_b16 v[216:217], %[w1h] offset:16384\n\t"
        "v_mfma_f32_16x16x32_bf16 v[112:115], v[84:87], v[176:179], v[112:115]\n\t"
        "ds_read_b64_tr_b16 v[218:219], %[w1h] offset:16896\n\t"
        "v_mfma_f32_16x16x32_bf16 v[128:131], v[88:91], v[176:179], v[128:131]\n\t"
        "ds_read_b128 v[132:135], %[xh] offset:16384\n\t"
        "ds_read_b128 v[136:139], %[xh] offset:16640\n\t"
        "v_mfma_f32_16x16x32_bf16 v[100:103], v[92:95], v[180:183], v[100:103]\n\t"
        "ds_read_b128 v[140:143], %[xh] offset:18432\n\t"
        "v_mfma_f32_16x16x32_bf16 v[116:119], v[96:99], v[180:183], v[116:119]\n\t"
        "ds_read_b128 v[144:147], %[xh] offset:18688\n\t"
        "v_mfma_f32_16x16x32_bf16 v[104:107], v[92:95], v[184:187], v[104:107]\n\t"
        "ds_read_b64_tr_b16 v[220:221], %[w0h] offset:18432\n\t"
        "ds_read_b64_tr_b16 v[222:223], %[w0h] offset:18944\n\t"
        "v_mfma_f32_16x16x32_bf16 v[120:123], v[96:99], v[184:187], v[120:123]\n\t"
        "ds_read_b64_tr_b16 v[224:225], %[w1h] offset:18432\n\t"
        "v_mfma_f32_16x16x32_bf16 v[108:111], v[92:95], v[188:191], v[108:111]\n\t"
        "ds_read_b64_tr_b16 v[226:227], %[w1h] offset:18944\n\t"
        "v_mfma_f32_16x16x32_bf16 v[124:127], v[96:99], v[188:191], v[124:127]\n\t"
        "ds_read_b128 v[148:151], %[xh] offset:20480\n\t"
        "ds_read_b128 v[152:155], %[xh] offset:20736\n\t"
        "v_mfma_f32_16x16x32_bf16 v[112:115], v[92:95], v[192:195], v[112:115]\n\t"
        "ds_read_b128 v[156:159], %[xh] offset:22528\n\t"
        "v_mfma_f32_16x16x32_bf16 v[128:131], v[96:99], v[192:195], v[128:131]\n\t"
        "ds_read_b128 v[160:163], %[xh] offset:22784\n\t"
        "s_waitcnt lgkmcnt(10)\n\t"
        "v_mfma_f32_16x16x32_bf16 v[100:103], v[212:215], v[132:135], v[100:103]\n\t"
        "v_mfma_f32_16x16x32_bf16 v[116:119], v[216:219], v[132:135], v[116:119]\n\t"
        "v_mfma_f32_16x16x32_bf16 v[104:107], v[212:215], v[136:139], v[104:107]\n\t"
        "v_mfma_f32_16x16x32_bf16 v[120:123], v[216:219], v[136:139], v[120:123]\n\t"
        "s_waitcnt lgkmcnt(0)\n\t"
        "s_barrier\n\t"
        "v_mfma_f32_16x16x32_bf16 v[108:111], v[212:215], v[140:143], v[108:111]\n\t"
        "ds_read_b64_tr_b16 v[196:197], %[w0] offset:0\n\t"
        "ds_read_b64_tr_b16 v[198:199], %[w0] offset:512\n\t"
        "v_mfma_f32_16x16x32_bf16 v[124:127], v[216:219], v[140:143], v[124:127]\n\t"
        "ds_read_b64_tr_b16 v[200:201], %[w1] offset:0\n\t"
        "v_mfma_f32_16x16x32_bf16 v[112:115], v[212:215], v[144:147], v[112:115]\n\t"
        "ds_read_b64_tr_b16 v[202:203], %[w1] offset:512\n\t"
        "v_mfma_f32_16x16x32_bf16 v[128:131], v[216:219], v[144:147], v[128:131]\n\t"
        "ds_read_b128 v[164:167], %[xh] offset:24576\n\t"
        "ds_read_b128 v[168:171], %[xh] offset:24832\n\t"
        "v_mfma_f32_16x16x32_bf16 v[100:103], v[220:223], v[148:151], v[100:103]\n\t"
        "ds_read_b128 v[172:175], %[xh] offset:26624\n\t"
        "v_mfma_f32_16x16x32_bf16 v[116:119], v[224:227], v[148:151], v[116:119]\n\t"
        "ds_read_b128 v[176:179], %[xh] offset:26880\n\t"
        "v_mfma_f32_16x16x32_bf16 v[104:107], v[220:223], v[152:155], v[104:107]\n\t"
        "ds_read_b64_tr_b16 v[204:205], %[w0] offset:2048\n\t"
        "ds_read_b64_tr_b16 v[206:207], %[w0] offset:2560\n\t"
        "v_mfma_f32_16x16x32_bf16 v[120:123], v[224:227], v[152:155], v[120:123]\n\t"
        "ds_read_b64_tr_b16 v[208:209], %[w1] offset:2048\n\t"
        "v_mfma_f32_16x16x32_bf16 v[108:111], v[220:223], v[156:159], v[108:111]\n\t"
        "ds_read_b64_tr_b16 v[210:211], %[w1] offset:2560\n\t"
        "v_mfma_f32_16x16x32_bf16 v[124:127], v[224:227], v[156:159], v[124:127]\n\t"
        "ds_read_b128 v[180:183], %[xh] offset:28672\n\t"
        "ds_read_b128 v[184:187], %[xh] offset:28928\n\t"
        "v_mfma_f32_16x16x32_bf16 v[112:115], v[220:223], v[160:163], v[112:115]\n\t"
        "ds_read_b128 v[188:191], %[xh] offset:30720\n\t"
        "v_mfma_f32_16x16x32_bf16 v[128:131], v[224:227], v[160:163], v[128:131]\n\t"
        "ds_read_b128 v[192:195], %[xh] offset:30976\n\t"
        "s_waitcnt lgkmcnt(10)\n\t"
        "v_mfma_f32_16x16x32_bf16 v[100:103], v[196:199], v[164:167], v[100:103]\n\t"
        "v_mfma_f32_16x16x32_bf16 v[116:119], v[200:203], v[164:167], v[116:119]\n\t"
        "v_mfma_f32_16x16x32_bf16 v[104:107], v[196:199], v[168:171], v[104:107]\n\t"
        "v_mfma_f32_16x16x32_bf16 v[120:123], v[200:203], v[168:171], v[120:123]\n\t"
        "s_waitcnt lgkmcnt(0)\n\t"
        "v_mfma_f32_16x16x32_bf16 v[108:111], v[196:199], v[172:175], v[108:111]\n\t"
        "v_mfma_f32_16x16x32_bf16 v[124:127], v[200:203], v[172:175], v[124:127]\n\t"
        "v_mfma_f32_16x16x32_bf16 v[112:115], v[196:199], v[176:179], v[112:115]\n\t"
        "v_mfma_f32_16x16x32_bf16 v[128:131], v[200:203], v[176:179], v[128:131]\n\t"
        "v_mfma_f32_16x16x32_bf16 v[100:103], v[204:207], v[180:183], v[100:103]\n\t"
        "v_mfma_f32_16x16x32_bf16 v[116:119], v[208:211], v[180:183], v[116:119]\n\t"
        "v_mfma_f32_16x16x32_bf16 v[104:107], v[204:207], v[184:187], v[104:107]\n\t"
        "v_mfma_f32_16x16x32_bf16 v[120:123], v[208:211], v[184:187], v[120:123]\n\t"
        "v_mfma_f32_16x16x32_bf16 v[108:111], v[204:207], v[188:191], v[108:111]\n\t"
        "v_mfma_f32_16x16x32_bf16 v[124:127], v[208:211], v[188:191], v[124:127]\n\t"
        "v_mfma_f32_16x16x32_bf16 v[112:115], v[204:207], v[192:195], v[112:115]\n\t"
        "v_mfma_f32_16x16x32_bf16 v[128:131], v[208:211], v[192:195], v[128:131]\n\t"
        "s_barrier\n\t"
        "ds_read_b64_tr_b16 v[132:133], %[z0] offset:0\n\t"
        "ds_read_b64 v[148:149], %[x0] offset:0\n\t"
        "ds_read_b64_tr_b16 v[134:135], %[z0] offset:2048\n\t"
        "ds_read_b64 v[150:151], %[x1] offset:0\n\t"
        "ds_read_b64_tr_b16 v[136:137], %[z1] offset:0\n\t"
        "ds_read_b64 v[152:153], %[x0] offset:4096\n\t"
        "ds_read_b64_tr_b16 v[138:139], %[z1] offset:2048\n\t"
        "ds_read_b64 v[154:155], %[x1] offset:4096\n\t"
        "ds_read_b64_tr_b16 v[140:141], %[z2] offset:0\n\t"
        "ds_read_b64 v[156:157], %[x0] offset:8192\n\t"
        "ds_read_b64_tr_b16 v[142:143], %[z2] offset:2048\n\t"
        "ds_read_b64 v[158:159], %[x1] offset:8192\n\t"
        "ds_read_b64_tr_b16 v[144:145], %[z3] offset:0\n\t"
        "ds_read_b64 v[160:161], %[x0] offset:12288\n\t"
        "ds_read_b64_tr_b16 v[146:147], %[z3] offset:2048\n\t"
        "ds_read_b64 v[162:163], %[x1] offset:12288\n\t"
        "s_waitcnt lgkmcnt(0)\n\t"
        : "=&{v[100:115]}"(b0), "=&{v[116:131]}"(b1), "=&{v[132:147]}"(zz), "=&{v[148:163]}"(xx)
        : "{v[20:35]}"(wr0), "{v[36:51]}"(wr1), "{v[52:67]}"(wr2), "{v[68:83]}"(wr3), "{v[84:99]}"(wr4), [w0] "v"(wa), [w1] "v"(wa ^ 32), [w0h] "v"(wa + 65536), [w1h] "v"((wa ^ 32) + 65536), [x] "v"(oa), [xh] "v"(oa + 108032),
          [z0] "v"(za[0]), [z1] "v"(za[1]), [z2] "v"(za[2]), [z3] "v"(za[3]), [x0] "v"(xa[0]), [x1] "v"(xa[1])
        : "memory", "v164", "v165", "v166", "v167", "v168", "v169", "v170", "v171", "v172", "v173", "v174", "v175", "v176", "v177", "v178", "v179", "v180", "v181", "v182", "v183", "v184", "v185", "v186", "v187", "v188", "v189", "v190", "v191", "v192", "v193", "v194", "v195", "v196", "v197", "v198", "v199", "v200", "v201", "v202", "v203", "v204", "v205", "v206", "v207", "v208", "v209", "v210", "v211", "v212", "v213", "v214", "v215", "v216", "v217", "v218", "v219", "v220", "v221", "v222", "v223", "v224", "v225", "v226", "v227");
#pragma unroll
        for (int ro = 0; ro < 4; ++ro)
#pragma unroll
            for (int dl = 0; dl < 2; ++dl) {
                const f32x16& bb = dl ? b1 : b0;
                const u32 z0 = zz[4 * ro + 2 * dl], z1 = zz[4 * ro + 2 * dl + 1], x0 = xx[4 * ro + 2 * dl], x1 = xx[4 * ro + 2 * dl + 1];
                const float o0 = dot2(x0, z0 & 0xffffu, bb[4 * ro]), o1 = dot2(x0, z0 & 0xffff0000u, bb[4 * ro + 1]);
                const float o2 = dot2(x1, z1 & 0xffffu, bb[4 * ro + 2]), o3 = dot2(x1, z1 & 0xffff0000u, bb[4 * ro + 3]);
                *(LAS u32x2*)(lds + (L_OUT - L_X + 4096 * ro) + (xa[dl] - ldsb)) = u32x2{pk2(o0, o1), pk2(o2, o3)};
            }
        BAR();
        store_rows();
    }
}

extern "C" void kernel_launch(void* const* d_in, const int* in_sizes, int n_in, void* d_out, int out_size, void* d_ws, size_t ws_size, hipStream_t stream) {
    Params p;
    p.x = (const u16*)d_in[0]; p.k = (const u16*)d_in[1]; p.v = (const u16*)d_in[2]; p.sk = (const u16*)d_in[3];
    p.ssmk = (const u16*)d_in[4]; p.ssmd = (const u16*)d_in[5]; p.wq = (const u16*)d_in[7]; p.wk = (const u16*)d_in[8];
    p.wv = (const u16*)d_in[9]; p.wo = (const u16*)d_in[10]; p.gw = (const u16*)d_in[11];
    p.out = (u16*)d_out; p.ws = (char*)d_ws;
    (void)hipFuncSetAttribute((const void*)fused_kernel, hipFuncAttributeMaxDynamicSharedMemorySize, LDS_BYTES);
    fused_kernel<<<256, NT, LDS_BYTES, stream>>>(p);
}
```
